```python
import torch

def _softmax(x, axis=-1):
    return torch.softmax(x, dim=axis)
def _gelu(x):
    return torch.nn.functional.gelu(x, approximate="tanh")
def _silu(x):
    return torch.nn.functional.silu(x)
def _sigmoid(x):
    return torch.sigmoid(x)
def _layernorm(x, axis=-1, eps=1e-5):
    m = x.mean(dim=axis, keepdim=True)
    v = x.var(dim=axis, keepdim=True, unbiased=False)
    return (x - m) / torch.sqrt(v + eps)
def _scatter_add(tgt, idx, src, axis=0):
    return tgt.clone().index_add_(axis, idx, src)
def _segment_sum(src, seg, n):
    out = torch.zeros((n,) + src.shape[1:], dtype=src.dtype)
    return out.index_add_(0, seg, src)
def _pad(x, pw):
    flat = tuple(v for pair in reversed(pw) for v in pair)
    return torch.nn.functional.pad(x, flat)

kernel_name = 'concat_heads__mha_block_cached__gqa_block_cached__near_ridge'

def setup_inputs():

    _w = (lambda a: (lambda t: t.to(torch.bfloat16) if t.is_floating_point() else t)(torch.as_tensor(a)))
    def _A_setup():
        B, Sq, D = 8, 208, 512
        H, Sk, Dh = 32, 512, 128
        return {
            "x": _w((lambda *s: torch.randn(*s, dtype=torch.bfloat16) * 0.8)(B, Sq, D)),
            "k_cache": _w((lambda *s: torch.randn(*s, dtype=torch.bfloat16) * 0.8)(B, H, Sk, Dh)),
            "v_cache": _w((lambda *s: torch.randn(*s, dtype=torch.bfloat16) * 0.05)(B, H, Sk, Dh)),
            "wq": _w((lambda *s: torch.randn(*s, dtype=torch.bfloat16) * 0.8)(D, H * Dh) * (D ** -0.5)),
            "wo": _w((lambda *s: torch.randn(*s, dtype=torch.bfloat16) * 0.05)(H * Dh, D) * ((H * Dh) ** -0.5)),
        }
    _A_in = _A_setup()

    def _B_setup():
        B, Sq, D = 8, 208, 512
        Hq, Hkv, Sk, Dh = 32, 32, 512, 128
        assert Hq % Hkv == 0, (Hq, Hkv)
        return {
            "x": _w((lambda *s: torch.randn(*s, dtype=torch.bfloat16) * 0.8)(B, Sq, D)),
            "k_cache": _w((lambda *s: torch.randn(*s, dtype=torch.bfloat16) * 0.8)(B, Hkv, Sk, Dh)),
            "v_cache": _w((lambda *s: torch.randn(*s, dtype=torch.bfloat16) * 0.05)(B, Hkv, Sk, Dh)),
            "wq": _w((lambda *s: torch.randn(*s, dtype=torch.bfloat16) * 0.8)(D, Hq * Dh) * (D ** -0.5)),
            "wo": _w((lambda *s: torch.randn(*s, dtype=torch.bfloat16) * 0.05)(Hq * Dh, D) * ((Hq * Dh) ** -0.5)),
        }
    _B_in = _B_setup()

    return {
        "A_x": _A_in["x"],
        "A_k_cache": _A_in["k_cache"],
        "A_v_cache": _A_in["v_cache"],
        "A_wq": _A_in["wq"],
        "A_wo": _A_in["wo"],
        "B_x": _B_in["x"],
        "B_k_cache": _B_in["k_cache"],
        "B_v_cache": _B_in["v_cache"],
        "B_wq": _B_in["wq"],
        "B_wo": _B_in["wo"],
    }

def reference(A_x, A_k_cache, A_v_cache, A_wq, A_wo, B_x, B_k_cache, B_v_cache, B_wq, B_wo):
    def _A_ref(x, k_cache, v_cache, wq, wo):
        B, Sq, D = x.shape
        H, Dh = 32, 128
        q = (lambda x, *d: x.permute(*d))((x @ wq).reshape(B, Sq, H, Dh), 0, 2, 1, 3)
        scores = torch.einsum("bhqd,bhkd->bhqk", q, k_cache) * (Dh ** -0.5)
        p = _softmax(scores, axis=-1)
        o = torch.einsum("bhqk,bhkd->bhqd", p, v_cache)
        return (lambda x, *d: x.permute(*d))(o, 0, 2, 1, 3).reshape(B, Sq, H * Dh) @ wo

    def _B_ref(x, k_cache, v_cache, wq, wo):
        B, Sq, D = x.shape
        Hq, Dh = 32, 128
        Hkv = k_cache.shape[1]
        rep = Hq // Hkv
        q = (lambda x, *d: x.permute(*d))((x @ wq).reshape(B, Sq, Hq, Dh), 0, 2, 1, 3)
        k = (lambda x, n, axis: torch.repeat_interleave(x, n, dim=axis))(k_cache, rep, axis=1)
        v = (lambda x, n, axis: torch.repeat_interleave(x, n, dim=axis))(v_cache, rep, axis=1)
        scores = torch.einsum("bhqd,bhkd->bhqk", q, k) * (Dh ** -0.5)
        p = _softmax(scores, axis=-1)
        o = torch.einsum("bhqk,bhkd->bhqd", p, v)
        return (lambda x, *d: x.permute(*d))(o, 0, 2, 1, 3).reshape(B, Sq, Hq * Dh) @ wo

    a = _A_ref(A_x, A_k_cache, A_v_cache, A_wq, A_wo)
    b = _B_ref(B_x, B_k_cache, B_v_cache, B_wq, B_wo)
    return torch.concatenate([a, b], axis=-1)
```

```cpp
#include <hip/hip_runtime.h>
#include <hip/hip_bf16.h>
typedef unsigned short u16;
__device__ __forceinline__ float bf2f(u16 v){ return __uint_as_float(((unsigned)v)<<16); }
__device__ __forceinline__ u16 f2bf(float f){ __hip_bfloat16 b = __float2bfloat16(f); return *(u16*)&b; }
__global__ void gemm_naive(const u16* A, const u16* B, u16* C, int M, int N, int K, int ldc, int coff){
  int n = blockIdx.x*blockDim.x+threadIdx.x; int m = blockIdx.y;
  if(n>=N) return;
  float s=0; for(int k=0;k<K;++k) s += bf2f(A[(size_t)m*K+k])*bf2f(B[(size_t)k*N+n]);
  C[(size_t)m*ldc+coff+n]=f2bf(s);
}
__global__ void attn_naive(const u16* Q, const u16* Kc, const u16* Vc, u16* O){
  int q = blockIdx.x, h = blockIdx.y, b = blockIdx.z; int t = threadIdx.x;
  __shared__ float s[512]; __shared__ float red[512]; __shared__ float qs[128];
  if(t<128) qs[t]=bf2f(Q[((size_t)(b*208+q))*4096+h*128+t]);
  __syncthreads();
  const u16* kr = Kc + (((size_t)(b*32+h))*512+t)*128;
  float a=0; for(int d=0;d<128;++d) a+=qs[d]*bf2f(kr[d]);
  a*=0.08838834764831845f;
  red[t]=a; __syncthreads();
  for(int o=256;o>0;o>>=1){ if(t<o) red[t]=fmaxf(red[t],red[t+o]); __syncthreads(); }
  float mx=red[0]; __syncthreads();
  float p=__expf(a-mx); s[t]=p; red[t]=p; __syncthreads();
  for(int o=256;o>0;o>>=1){ if(t<o) red[t]+=red[t+o]; __syncthreads(); }
  float l=red[0];
  if(t<128){ const u16* vr = Vc + ((size_t)(b*32+h))*512*128 + t; float o=0; for(int k=0;k<512;++k) o+=s[k]*bf2f(vr[(size_t)k*128]); O[((size_t)(b*208+q))*4096+h*128+t]=f2bf(o/l); }
}
extern "C" void kernel_launch(void* const* d_in, const int* in_sizes, int n_in, void* d_out, int out_size, void* d_ws, size_t ws_size, hipStream_t stream) {
  u16* Q=(u16*)d_ws; u16* O=Q+(size_t)1664*4096;
  for(int br=0;br<2;++br){
    const u16* x=(const u16*)d_in[br*5+0]; const u16* k=(const u16*)d_in[br*5+1]; const u16* v=(const u16*)d_in[br*5+2]; const u16* wq=(const u16*)d_in[br*5+3]; const u16* wo=(const u16*)d_in[br*5+4];
    gemm_naive<<<dim3(16,1664),256,0,stream>>>(x,wq,Q,1664,4096,512,4096,0);
    attn_naive<<<dim3(208,32,8),512,0,stream>>>(Q,k,v,O);
    gemm_naive<<<dim3(2,1664),256,0,stream>>>(O,wo,(u16*)d_out,1664,512,4096,1024,br*512);
  }
}
```

```cpp
#include <hip/hip_runtime.h>
#include <hip/hip_bf16.h>

typedef unsigned short u16;
typedef unsigned int u32;
typedef __bf16 bf16x8 __attribute__((ext_vector_type(8)));
typedef __bf16 bf16x4 __attribute__((ext_vector_type(4)));
typedef __bf16 bf16x2 __attribute__((ext_vector_type(2)));
typedef float f32x4 __attribute__((ext_vector_type(4)));
typedef float f32x2 __attribute__((ext_vector_type(2)));
typedef u32 u32x4 __attribute__((ext_vector_type(4)));
#define LAS __attribute__((address_space(3)))
#define DI __device__ __forceinline__
typedef __amdgpu_buffer_rsrc_t rsrc_t;

#define MFMA(a, b, c) __builtin_amdgcn_mfma_f32_16x16x32_bf16((a), (b), (c), 0, 0, 0)
#define WAIT_BAR(N) asm volatile("s_waitcnt vmcnt(" #N ") lgkmcnt(0)\n\ts_barrier" ::: "memory")

DI rsrc_t mk_rsrc(const void* p, u32 bytes) { return __builtin_amdgcn_make_buffer_rsrc((void*)p, (short)0, (int)bytes, 0x00020000); }
DI void dma_nt(u32 lds, u32 voff, rsrc_t rs, u32 soff) {
  asm volatile("s_mov_b32 m0, %0\n\ts_nop 0\n\tbuffer_load_dwordx4 %1, %2, %3 offen nt lds" ::"s"(lds), "v"(voff), "s"(rs), "s"(soff));
}
DI void dma_df(u32 lds, u32 voff, rsrc_t rs, u32 soff) {
  asm volatile("s_mov_b32 m0, %0\n\ts_nop 0\n\tbuffer_load_dwordx4 %1, %2, %3 offen lds" ::"s"(lds), "v"(voff), "s"(rs), "s"(soff));
}
#define POL_XW 1
#define POL_O2 0
#define POL_WO 1
DI void dma_xw(u32 lds, u32 voff, rsrc_t rs, u32 soff) { if (POL_XW) dma_nt(lds, voff, rs, soff); else dma_df(lds, voff, rs, soff); }
DI void dma_o2(u32 lds, u32 voff, rsrc_t rs, u32 soff) { if (POL_O2) dma_nt(lds, voff, rs, soff); else dma_df(lds, voff, rs, soff); }
DI void dma_wo(u32 lds, u32 voff, rsrc_t rs, u32 soff) { if (POL_WO) dma_nt(lds, voff, rs, soff); else dma_df(lds, voff, rs, soff); }

DI bf16x8 lds128(u32 a) { return *(const LAS bf16x8*)(size_t)a; }
DI bf16x4 ldstr(u32 a) { return __builtin_amdgcn_ds_read_tr16_b64_v4bf16((LAS bf16x4*)(size_t)a); }
DI bf16x8 ldstr2(u32 a0, u32 a1) { bf16x4 lo = ldstr(a0), hi = ldstr(a1); return __builtin_shufflevector(lo, hi, 0, 1, 2, 3, 4, 5, 6, 7); }
DI u32 cvtpk(float lo, float hi) { f32x2 v = {lo, hi}; return __builtin_bit_cast(u32, __builtin_convertvector(v, bf16x2)); }
DI float xsum_g(float v) {
  auto a = __builtin_amdgcn_permlane16_swap(__float_as_uint(v), __float_as_uint(v), false, false);
  v = __uint_as_float(a[0]) + __uint_as_float(a[1]);
  auto b = __builtin_amdgcn_permlane32_swap(__float_as_uint(v), __float_as_uint(v), false, false);
  return __uint_as_float(b[0]) + __uint_as_float(b[1]);
}
DI float xmax_g(float v) {
  auto a = __builtin_amdgcn_permlane16_swap(__float_as_uint(v), __float_as_uint(v), false, false);
  v = fmaxf(__uint_as_float(a[0]), __uint_as_float(a[1]));
  auto b = __builtin_amdgcn_permlane32_swap(__float_as_uint(v), __float_as_uint(v), false, false);
  return fmaxf(__uint_as_float(b[0]), __uint_as_float(b[1]));
}

constexpr float QSCALE = 0.08838834764831845f * 1.4426950408889634f;
#define GAP() __builtin_amdgcn_sched_barrier(0)
#define PIN(x) asm volatile("" : "+v"(x))
#define ZERO4 (f32x4{0.f, 0.f, 0.f, 0.f})
template <int N> struct IC { static constexpr int v = N; };
template <int B, int E, class F> DI void sfor(F&& f) { if constexpr (B < E) { f(IC<B>{}); sfor<B + 1, E>(f); } }

#define STAMP(i)
#define ROT 3
#define ROT2 1
#define WOPF 0
#define PART_BF16 1
#define PRIO 0
#define KVIDX (b * 32 + h)
#define PD 4
constexpr u32 VRING = 65536, XBUF = 65536, XSZ = 26624, K0A = 131072, V0A = 147456, V0B = 122880, Q12 = 155648, P12 = 131072, FLG = 139264;
typedef u32 u32x2 __attribute__((ext_vector_type(2)));

DI float max8(const f32x4& a, const f32x4& b) { return fmaxf(fmaxf(fmaxf(a[0], a[1]), fmaxf(a[2], a[3])), fmaxf(fmaxf(b[0], b[1]), fmaxf(b[2], b[3]))); }
DI bf16x8 asb(const u32x4& w) { return __builtin_bit_cast(bf16x8, w); }
DI bf16x8 kfrag(const u32 (&ka)[2], int f, u32 off) { return lds128(ka[f & 1] + (((f >> 1) & 1) * 4096 + (f >> 2) * 2048 + off)); }
DI bf16x8 vfrag(const u32 (&va)[4], int i, u32 off) { const u32 a = va[((i >> 1) + 1) & 3] + ((i & 1) * 4096 + off); return ldstr2(a, a + 2048); }

template <int NT, int I0>
DI void safe_block(u32 (&ka)[2], u32 (&va)[4], const bf16x8 (&qf)[4], f32x4 (&o)[NT], float& m, float& l) {
  f32x4 s[2];
#pragma unroll
  for (int f = 0; f < 8; ++f) { s[f >> 2] = MFMA(kfrag(ka, f, 0), qf[f & 3], (f & 3) ? s[f >> 2] : ZERO4); GAP(); }
  const float mx = fmaxf(xmax_g(max8(s[0], s[1])), m);
  const float al = __builtin_amdgcn_exp2f(m - mx);
  m = mx;
  float e[8];
#pragma unroll
  for (int i = 0; i < 8; ++i) e[i] = __builtin_amdgcn_exp2f(s[i >> 2][i & 3] - mx);
  l = l * al + (((e[0] + e[1]) + (e[2] + e[3])) + ((e[4] + e[5]) + (e[6] + e[7])));
  u32x4 w = {cvtpk(e[0], e[1]), cvtpk(e[2], e[3]), cvtpk(e[4], e[5]), cvtpk(e[6], e[7])};
  GAP();
#pragma unroll
  for (int i = 0; i < NT; ++i) { o[i] *= al; PIN(o[i]); o[i] = MFMA(vfrag(va, I0 + i, 0), asb(w), o[i]); PIN(o[i]); GAP(); }
}

constexpr int EP(int k) { return 14 + 2 * k; }
constexpr int CW(int q, int w) { return w < 2 ? 43 + 2 * q : (q == 0 ? 49 + 2 * (w - 2) : (q == 1 ? 53 + 2 * (w - 2) : 59 + 2 * (w - 2))); }

template <int POS>
DI void sm_ops(f32x4 (&s0)[3], f32x4 (&s1)[3], u32x4 (&p)[3], float (&l)[4]) {
  sfor<0, 24>([&](auto kc) {
    constexpr int k = decltype(kc)::v, q = (k % 12) / 4, r = k & 3;
    if constexpr (EP(k) + 1 == POS) { l[q] += (k < 12) ? s0[q][r] : s1[q][r]; PIN(l[q]); }
    if constexpr (EP(k) == POS) {
      if constexpr (k < 12) { s0[q][r] = __builtin_amdgcn_exp2f(s0[q][r]); PIN(s0[q]); }
      else { s1[q][r] = __builtin_amdgcn_exp2f(s1[q][r]); PIN(s1[q]); }
    }
  });
  sfor<0, 12>([&](auto qc) {
    constexpr int q = decltype(qc)::v / 4, w = decltype(qc)::v & 3;
    if constexpr (CW(q, w) == POS) {
      if constexpr (w < 2) p[q][w] = cvtpk(s0[q][2 * w], s0[q][2 * w + 1]); else p[q][w] = cvtpk(s1[q][2 * w - 4], s1[q][2 * w - 3]);
      PIN(p[q]);
    }
  });
}
#define SET_M0(base, imm) asm volatile("s_add_u32 m0, %0, %1" ::"s"(base), "i"(imm) : "scc")
#define DMA_LD(voff, rs, soff, imm) asm volatile("buffer_load_dwordx4 %0, %1, %2 offen offset:%3 nt lds" ::"v"(voff), "s"(rs), "s"(soff), "i"(imm))
#define DMA_XW DMA_LD
#define DMA_DF(voff, rs, soff, imm) asm volatile("buffer_load_dwordx4 %0, %1, %2 offen offset:%3 lds" ::"v"(voff), "s"(rs), "s"(soff), "i"(imm))
#define DMA_WO DMA_LD
#define DMA_O2 DMA_DF
#define PINS(x) asm volatile("" : "+s"(x))
#define SETPRIO(n)

__global__ __launch_bounds__(512, 2) void k1_qattn(const u16* __restrict__ xA, const u16* __restrict__ kA, const u16* __restrict__ vA, const u16* __restrict__ wqA,
                                                   const u16* __restrict__ xB, const u16* __restrict__ kB, const u16* __restrict__ vB, const u16* __restrict__ wqB,
                                                   const u16* __restrict__ woA, const u16* __restrict__ woB, u16* __restrict__ ows, u32* __restrict__ cnts) {
  extern __shared__ __attribute__((aligned(16))) char smem[];
  const u32 bid = blockIdx.x, xcd = bid & 7, rr = bid >> 3;
  const u32 br = xcd & 1, b = rr & 7, hpl = rr >> 3, hp = (xcd >> 1) * 4 + hpl;
  const u32 lane = threadIdx.x & 63;
  const u32 wid = __builtin_amdgcn_readfirstlane(threadIdx.x >> 6);
  const u32 hh = wid >> 2, wi = wid & 3, h = hp * 2 + hh;
  constexpr u32 RM = ROT == 3 ? 3 : (ROT == 4 ? 1 : 7);
  const u32 c0 = ROT == 1 ? (b + 2 * hpl) & 7 : (ROT == 2 ? ((b ^ hpl) & 1) * 4 : (ROT == 3 ? (b + hpl) & 3 : (ROT == 4 ? (b + hpl) & 1 : 0)));
  auto chunk_of = [&](u32 c) { return (c & ~RM) | ((c + c0) & RM); };
  const u32 lds0 = (u32)(size_t)(LAS char*)smem;
  const rsrc_t rx = mk_rsrc((br ? xB : xA) + (size_t)b * 208 * 512, 208 * 512 * 2);
  const rsrc_t rw = mk_rsrc(br ? wqB : wqA, 512 * 4096 * 2);
  const rsrc_t rk = mk_rsrc((br ? kB : kA) + (size_t)(KVIDX) * 512 * 128, 512 * 128 * 2);
  const rsrc_t rv = mk_rsrc((br ? vB : vA) + (size_t)(KVIDX) * 512 * 128, 512 * 128 * 2);
  STAMP(0);
  asm volatile("s_nop 4");

  const u32 g = lane >> 4, r16 = lane & 15, q4 = r16 >> 2, p4 = lane & 3;
  const u32 j3 = lane >> 3, j7 = lane & 7, j4 = lane >> 4;
  const u32 voffX = j3 * 1024 + ((j7 ^ ((wid & 1) * 4 + j4)) * 16);
  const u32 voffW = j3 * 8192 + ((j7 ^ ((j4 & 1) | ((wid & 1) << 2))) * 16);
  const u32 voff = j3 * 256 + (((j7 >> 1) ^ j4) * 32) + (lane & 1) * 16;
  const u32 kvl = lds0 + hh * 8192 + wi * 1024;
  const u32 kvs = wi * 2048;
  auto kv_piece = [&](rsrc_t rs, u32 step, u32 lb, int hf) { dma_nt(lb + hf * 4096, voff, rs, step * 8192 + kvs + hf * 128); };
  const u32 swb = wi * 65536 + h * 256;
  u32 jw = 0, jx = 0, sw = c0 * 524288 + swb, lw = kvl + 16384;
  u32 sx_ = wid * 8192 + c0 * 128, lx = lds0 + XBUF + wid * 1024, lx3 = wid < 2 ? lx + 24576 : lds0 + Q12 + wid * 1024;
  const u32 dx3 = wid < 2 ? XSZ : 0;
  u32 voffXi[4];
#pragma unroll
  for (int i = 0; i < 4; ++i) voffXi[i] = voffX + i * 65536;
  auto w_m0 = [&](int hf) { SET_M0(lw, hf * 3968); };
  auto w_ld = [&](int hf) { DMA_XW(voffW, rw, sw, hf * 128); };
  auto w_adv = [&](int ph) { if (ph == 0) { ++jw; sw = chunk_of(jw >> 1) * 524288 + (jw & 1) * 262144 + swb; PINS(sw); PINS(jw); } else { lw = (lw + 16384) & 0xffffu; PINS(lw); } };
  auto x_m0 = [&](int i) { if (i < 3) SET_M0(lx, i * 8192); else SET_M0(lx3, 0); };
  auto x_ld = [&](int i) { DMA_XW(voffXi[i], rx, sx_, 0); };
  auto x_adv = [&](u32 c) {
    ++jx; sx_ = wid * 8192 + chunk_of(jx) * 128; PINS(jx);
    lx += (c & 1) ? (u32)-XSZ : XSZ; lx3 += (c & 1) ? (u32)-dx3 : dx3;
    PINS(sx_); PINS(lx); PINS(lx3);
  };
  auto nop1 = [&]() { asm volatile("s_nop 0"); };
  const u32 v0l = lds0 + (hh ? V0B : V0A) + wi * 1024;
  w_m0(0); nop1(); w_ld(0); w_m0(1); nop1(); w_ld(1); w_adv(0); w_adv(1);
#pragma unroll
  for (int i = 0; i < 4; ++i) { x_m0(i); nop1(); x_ld(i); }
  x_adv(0);
  w_m0(0); nop1(); w_ld(0); w_m0(1); nop1(); w_ld(1); w_adv(0); w_adv(1);

  u32 xa[3], wa[4];
  xa[0] = lds0 + XBUF + wi * 6144 + r16 * 128 + ((g ^ (r16 >> 1)) * 16);
  xa[1] = xa[0] ^ 64u;
  xa[2] = xa[0] + (4 - wi) * 6144;
  wa[0] = lds0 + 16384 + hh * 8192 + (8 * g + q4) * 128 + ((((2 * (p4 >> 1)) | (wi & 1) | ((wi >> 1) << 2)) ^ ((q4 >> 1) | ((g & 1) << 2))) * 16) + (p4 & 1) * 8;
#pragma unroll
  for (int j = 1; j < 4; ++j) wa[j] = wa[0] ^ (u32)(((j & 1) << 4) | ((j >> 1) << 6));
  f32x4 o[3][8], ox[2];
  auto wread = [&](int n, int hi) { return ldstr(wa[(((n & 7) >> 1) + 1) & 3] + ((n & 1) * 4096 + hi * 512)); };
  bf16x8 xc[4];
  bf16x4 wcl[3], wch[3];
  auto qchunk = [&](auto kindc, u32 c) {
    constexpr int KIND = decltype(kindc)::v;
    bf16x8 xf[2][4];
    bf16x4 wl[19], wh[19];
    u32 x13;
#pragma unroll
    for (int q = 0; q < 4; ++q) xf[0][q] = xc[q];
#pragma unroll
    for (int q = 0; q < 3; ++q) { wl[q] = wcl[q]; wh[q] = wch[q]; }
    const u32 dw = (c & 1) ? (u32)-49152 : 16384u, dx = (c & 1) ? (u32)-XSZ : XSZ;
    GAP();
    sfor<0, 16>([&](auto nc) {
      constexpr int n = decltype(nc)::v, i = n & 7;
      constexpr bool RD = (n + 3 < 16 || KIND != 4);
      const bf16x8 wf = __builtin_shufflevector(wl[n], wh[n], 0, 1, 2, 3, 4, 5, 6, 7);
      auto dma_op = [&](auto kc) {
        constexpr int k = decltype(kc)::v;
        constexpr bool EV = n >= 4 && n < 12;
        if constexpr (KIND == 0 && n < 4) {
          constexpr int z = k - 16;
          if constexpr (z == 0) w_m0(0); if constexpr (z == 1) { w_ld(0); w_m0(1); } if constexpr (z == 2) { w_ld(1); w_adv(0); w_adv(1); x_m0(0); }
          if constexpr (z >= 3 && z < 6) { x_ld(z - 3); x_m0(z - 2); } if constexpr (z == 6) { x_ld(3); x_adv(1); }
        } else if constexpr (n >= 4) {
          constexpr bool W = EV ? KIND <= 3 : KIND <= 2;
          if constexpr (W) {
            if constexpr (k == 0) w_m0(0); if constexpr (k == 1) w_ld(0); if constexpr (k == 2) w_m0(1); if constexpr (k == 3) w_ld(1);
            if constexpr (k == 4) w_adv(0); if constexpr (k == 5) w_adv(1);
          }
          if constexpr (EV && KIND <= 2) {
            if constexpr (k >= 6 && k < 14) { if constexpr (k & 1) x_ld((k - 6) / 2); else x_m0((k - 6) / 2); }
            if constexpr (k == 14) x_adv(c);
          }
          if constexpr (!EV && KIND == 0) {
            if constexpr (k == 6) { kv_piece(rk, 0, kvl + K0A, 0); kv_piece(rk, 0, kvl + K0A, 1); }
            if constexpr (k == 7) { kv_piece(rv, 0, v0l, 0); kv_piece(rv, 0, v0l, 1); }
          }
          if constexpr (EV && KIND == 3 && (k == 6 || k == 7)) kv_piece(rv, 1, kvl + VRING, k & 1);
          if constexpr (!EV && KIND == 3 && k < 2) kv_piece(rk, 1, kvl + 16384, k & 1);
          if constexpr (EV && KIND == 4 && k < 2) kv_piece(rk, 2, kvl + 32768, k & 1);
          if constexpr (EV && KIND == 4 && k >= 2 && k < 6) kv_piece(rv, k / 2 + 1, kvl + VRING + (k / 2) * 16384, k & 1);
          if constexpr (!EV && KIND == 4 && k < 2) kv_piece(rk, 3, kvl + 49152, k & 1);
        }
      };
      constexpr int kb = n < 4 ? 16 + 2 * n : 2 * ((n - 4) & 7);
      o[0][i] = MFMA(wf, xf[n >> 3][0], (KIND == 0 && n < 8) ? ZERO4 : o[0][i]);
      GAP();
      if constexpr (RD) { wl[n + 3] = wread(n + 3, 0); wh[n + 3] = wread(n + 3, 1); }
      GAP();
      o[1][i] = MFMA(wf, xf[n >> 3][1], (KIND == 0 && n < 8) ? ZERO4 : o[1][i]);
      GAP();
      if constexpr (n < 3) xf[1][n] = lds128(xa[1] + n * 2048);
      if constexpr (KIND != 4 && n >= 12 && n < 15) xc[n - 12] = lds128(xa[0] + (n - 12) * 2048);
      dma_op(IC<kb>{});
      GAP();
      if constexpr (n == 0) { x13 = xa[2] ^ 64u; PIN(x13); }
      if constexpr (n == 4 || (n == 12 && KIND != 4)) { wa[0] += (n == 4 ? dw : 16384u); wa[1] += (n == 4 ? dw : 16384u); PIN(wa[0]); PIN(wa[1]); }
      if constexpr (n == 6 && KIND != 4) { xa[0] += dx; xa[1] += dx; PIN(xa[0]); PIN(xa[1]); }
      GAP();
      o[2][i] = MFMA(wf, xf[n >> 3][2], (KIND == 0 && n < 8) ? ZERO4 : o[2][i]);
      GAP();
      if constexpr (n == 1) xf[1][3] = lds128(x13);
      if constexpr (KIND != 4 && n == 15) xc[3] = lds128(xa[2]);
      dma_op(IC<kb + 1>{});
      GAP();
      if constexpr (n == 4 || (n == 12 && KIND != 4)) { wa[2] += (n == 4 ? dw : 16384u); wa[3] += (n == 4 ? dw : 16384u); PIN(wa[2]); PIN(wa[3]); }
      if constexpr (n == 6 && KIND != 4) { xa[2] += dx; PIN(xa[2]); }
      GAP();
      if constexpr (i >= 6) { ox[i - 6] = MFMA(wf, xf[n >> 3][3], (KIND == 0 && n < 8) ? ZERO4 : ox[i - 6]); GAP(); }
      if constexpr (n == 3 || n == 11) {
        if constexpr (KIND == 4 && n == 11) asm volatile("s_barrier" ::: "memory");
        else if constexpr (KIND == 1) asm volatile("s_waitcnt vmcnt(10) lgkmcnt(2)\n\ts_barrier" ::: "memory");
        else if constexpr ((KIND == 3 && n == 11) || KIND == 4) asm volatile("s_waitcnt vmcnt(4) lgkmcnt(2)\n\ts_barrier" ::: "memory");
        else asm volatile("s_waitcnt vmcnt(6) lgkmcnt(2)\n\ts_barrier" ::: "memory");
        GAP();
      }
    });
#pragma unroll
    for (int q = 0; q < 3; ++q) { wcl[q] = wl[16 + q]; wch[q] = wh[16 + q]; }
  };
  STAMP(1);
  WAIT_BAR(2);
  STAMP(2);
#pragma unroll
  for (int q = 0; q < 3; ++q) { wcl[q] = wread(q, 0); wch[q] = wread(q, 1); xc[q] = lds128(xa[0] + q * 2048); }
  xc[3] = lds128(xa[2]);
  qchunk(IC<0>{}, 0);
  STAMP(3);
  qchunk(IC<1>{}, 1);
#pragma unroll 1
  for (u32 c = 2; c < 6; ++c) qchunk(IC<2>{}, c);
  STAMP(4);
  qchunk(IC<3>{}, 6);
  qchunk(IC<4>{}, 7);
  STAMP(5);
  constexpr int IPOS[8] = {6, 0, 2, 4, 7, 1, 3, 5};
  const bool odd = wi & 1;
  bf16x8 qf[3][4];
#pragma unroll
  for (int q = 0; q < 3; ++q)
#pragma unroll
    for (int u = 0; u < 4; ++u) {
      const f32x4 a = o[q][IPOS[2 * u]] * QSCALE, bb = o[q][IPOS[2 * u + 1]] * QSCALE;
      const u32 a0 = cvtpk(a[0], a[1]), a1 = cvtpk(a[2], a[3]), b0 = cvtpk(bb[0], bb[1]), b1 = cvtpk(bb[2], bb[3]);
      auto s0 = __builtin_amdgcn_permlane16_swap(odd ? b0 : a0, odd ? a0 : b0, false, false);
      auto s1 = __builtin_amdgcn_permlane16_swap(odd ? b1 : a1, odd ? a1 : b1, false, false);
      qf[q][u] = asb(u32x4{s0[0], s1[0], s0[1], s1[1]});
    }
  u32 la16 = lane * 16;
  const u32 q12s = lds0 + Q12 + hh * 4096 + (wi >> 1) * 1024;
#pragma unroll
  for (int hf = 0; hf < 2; ++hf) {
    const f32x4 a = ox[hf] * QSCALE;
    *(LAS u32x2*)(size_t)(q12s + hf * 2048 + ((2 * (g >> 1) + (wi & 1)) * 16 + r16) * 16 + (g & 1) * 8) = u32x2{cvtpk(a[0], a[1]), cvtpk(a[2], a[3])};
  }
  auto q12frag = [&](int u) { return lds128(la16 + ((q12s ^ (u32)((u & 1) << 10)) + (u >> 1) * 2048)); };
  u32 ka[2], va[4];
  ka[0] = lds0 + hh * 8192 + r16 * 128 + ((((g >> 1) | ((wi >> 1) << 1)) ^ ((r16 >> 1) & 3)) * 32) + (g & 1) * 16;
  ka[1] = ka[0] ^ 64u;
  va[0] = lds0 + (hh ? V0B : V0A) - 16384 + (4 * g + q4) * 128 + ((((q4 >> 1) | ((g & 1) << 1)) ^ wi) * 32) + p4 * 8;
#pragma unroll
  for (int j = 1; j < 4; ++j) va[j] = va[0] ^ (u32)(j << 5);
  asm volatile("s_waitcnt lgkmcnt(0)\n\ts_barrier" ::: "memory");
  GAP();

  const u32 p12s = lds0 + P12 + hh * 4096;
  f32x4 negm[3], s0[3], s1[3];
  float m12;
  u32x4 p[3] = {}, px0;
  float l[4] = {0.f, 0.f, 0.f, 0.f};
  {
    const u32 k0[2] = {ka[0] + K0A, ka[1] + K0A};
    f32x4 t0, t1;
    bf16x8 kf[8], qx[4];
#pragma unroll
    for (int f = 0; f < 4; ++f) { kf[f] = kfrag(k0, f, 0); qx[f] = q12frag(f); }
    GAP();
    sfor<0, 8>([&](auto fc) {
      constexpr int f = decltype(fc)::v;
      if constexpr (f < 4) kf[f + 4] = kfrag(k0, f + 4, 0);
#pragma unroll
      for (int q = 0; q < 3; ++q) {
        if (f < 4) s0[q] = MFMA(kf[f], qf[q][f & 3], (f & 3) ? s0[q] : ZERO4);
        else s1[q] = MFMA(kf[f], qf[q][f & 3], (f & 3) ? s1[q] : ZERO4);
      }
      if (f < 4) t0 = MFMA(kf[f], qx[f & 3], (f & 3) ? t0 : ZERO4);
      else t1 = MFMA(kf[f], qx[f & 3], (f & 3) ? t1 : ZERO4);
      GAP();
    });
#pragma unroll
    for (int q = 0; q < 3; ++q) {
      const float mx = xmax_g(max8(s0[q], s1[q]));
      negm[q] = f32x4{-mx, -mx, -mx, -mx};
      s0[q] -= mx;
      s1[q] -= mx;
    }
    m12 = xmax_g(max8(t0, t1));
    float e[8];
#pragma unroll
    for (int i = 0; i < 8; ++i) e[i] = __builtin_amdgcn_exp2f((i < 4 ? t0[i & 3] : t1[i & 3]) - m12);
    l[3] = wi == 0 ? ((e[0] + e[1]) + (e[2] + e[3])) + ((e[4] + e[5]) + (e[6] + e[7])) : 0.f;
    px0 = u32x4{cvtpk(e[0], e[1]), cvtpk(e[2], e[3]), cvtpk(e[4], e[5]), cvtpk(e[6], e[7])};
    sfor<0, 50>([&](auto pc) { sm_ops<decltype(pc)::v>(s0, s1, p, l); });
  }
  STAMP(6);
  WAIT_BAR(8);
  STAMP(7);
  const u32 kt12 = (wi & 1) * 2048;
  bf16x8 kfc[3];
#pragma unroll
  for (int f = 0; f < 3; ++f) kfc[f] = kfrag(ka, f, 16384);
  if (wi < 2) {
    const u32 kx[2] = {ka[0] + kt12, ka[1] + kt12};
    f32x4 sy;
#pragma unroll
    for (int u = 0; u < 4; ++u) sy = MFMA(kfrag(kx, u, 16384), q12frag(u), u ? sy : ZERO4);
#pragma unroll
    for (int r = 0; r < 4; ++r) sy[r] = __builtin_amdgcn_exp2f(sy[r] - m12);
    l[3] += (sy[0] + sy[1]) + (sy[2] + sy[3]);
    *(LAS u32x2*)(size_t)(la16 + (p12s + 1024 + (wi & 1) * 8)) = u32x2{cvtpk(sy[0], sy[1]), cvtpk(sy[2], sy[3])};
  }
  const rsrc_t rk2 = mk_rsrc((br ? kB : kA) + (size_t)(KVIDX) * 512 * 128 + 4096, 512 * 128 * 2 - 8192);
  u32 so = 3 * 8192 + kvs;
  u32 lhk = kvl, lhv = kvl + VRING + 32768;
  u32 kxs = 32768 + kt12 + (wi >> 1) * 16384;
  u32 qs0 = q12s - p12s, qs1 = (q12s ^ 1024u) - p12s;
  u32 pa = la16 + p12s;
  const u32 pwo = (wi >> 1) * 1024 + (wi & 1) * 8;
  f32x4 sx;
  auto step = [&](auto oddc, auto firstc) {
    constexpr bool ODD = decltype(oddc)::v, FIRST = decltype(firstc)::v;
    constexpr u32 SO = ODD ? 16384 : 0, SN = ODD ? 0 : 16384;
    bf16x8 kf[8], kx[4], qx[4];
    bf16x4 vl[8], vh[8];
#pragma unroll
    for (int f = 0; f < 3; ++f) kf[f] = kfc[f];
    f32x4 n0[3], n1[3];
    u32 pxw[2];
    u32x4 px;
    u32 kxa[2], qa[2], pw;
    auto vread = [&](int i, int hi) {
      const u32 a = va[((i >> 1) + 1) & 3] + ((i & 1) * 4096 + SO + hi * 2048);
      if (hi) vh[i] = ldstr(a); else vl[i] = ldstr(a);
    };
    auto vfr = [&](int i) { return __builtin_shufflevector(vl[i], vh[i], 0, 1, 2, 3, 4, 5, 6, 7); };
    SETPRIO(1);
    GAP();
    sfor<0, 50>([&](auto jc) {
      constexpr int j = decltype(jc)::v;
      if constexpr (j < 24) {
        constexpr int f = j / 3, q = j % 3, u = f & 3;
        if constexpr (f < 4) n0[q] = MFMA(kf[f], qf[q][u], u == 0 ? negm[q] : n0[q]);
        else n1[q] = MFMA(kf[f], qf[q][u], u == 0 ? negm[q] : n1[q]);
      } else if constexpr (j < 48) {
        constexpr int i = j < 42 ? (j - 24) / 3 : 6 + (j & 1), q = j < 42 ? (j - 24) % 3 : (j - 42) / 2;
        o[q][i] = MFMA(vfr(i), asb(p[q]), FIRST ? ZERO4 : o[q][i]);
      } else {
        ox[j - 48] = MFMA(vfr(j - 42), asb(px), FIRST ? ZERO4 : ox[j - 48]);
      }
      GAP();
      if constexpr (j == 0) SET_M0(lhk, SN);
      if constexpr (j == 1) DMA_LD(voff, rk2, so, 0);
      if constexpr (j == 3) SET_M0(lhk, SN + 3968);
      if constexpr (j == 4) DMA_LD(voff, rk2, so, 128);
      if constexpr (j < 15 && j % 3 == 2) kf[3 + j / 3] = kfrag(ka, 3 + j / 3, SO);
      if constexpr (j == 12) SETPRIO(0);
      if constexpr (j >= 16 && j < 36 && (j - 16) % 3 < 2) vread((j - 16) / 3, (j - 16) % 3);
      if constexpr (j == 36 || j == 37) vread(7, j - 36);
      if constexpr (j >= 43 && (j - 43) % 3 == 0) kfc[(j - 43) / 3] = kfrag(ka, (j - 43) / 3, SN);
      if constexpr (j == 40) { if constexpr (FIRST) px = px0; else px = *(const LAS u32x4*)(size_t)(pa + (ODD ? 0 : 1024)); }
      if constexpr (!FIRST) {
        if constexpr (j == (ODD ? 44 : 24)) SET_M0(lhv, SN);
        if constexpr (j == (ODD ? 45 : 27)) DMA_LD(voff, rv, so, 0);
        if constexpr (j == (ODD ? 47 : 30)) SET_M0(lhv, SN + 3968);
        if constexpr (j == (ODD ? 48 : 33)) DMA_LD(voff, rv, so, 128);
      }
      if constexpr (j == 48) { so += 8192; PINS(so); }
      if constexpr (ODD) {
        if constexpr (j == 18 && !FIRST) *(LAS u32x2*)(size_t)pw = u32x2{pxw[0], pxw[1]};
        if constexpr (j == 24) kx[0] = kfrag(kxa, 0, 0);
        if constexpr (j == 27) qx[0] = lds128(qa[0]);
        if constexpr (j == 30) kx[1] = kfrag(kxa, 1, 0);
        if constexpr (j == 33) qx[1] = lds128(qa[1]);
        if constexpr (j == 38) kx[2] = kfrag(kxa, 2, 0);
        if constexpr (j == 39) qx[2] = lds128(qa[0] + 2048);
        if constexpr (j == 41) kx[3] = kfrag(kxa, 3, 0);
        if constexpr (j == 42) qx[3] = lds128(qa[1] + 2048);
      } else {
        if constexpr (j == 38) SETPRIO(2);
        if constexpr (j == 39) { lhk ^= 32768u; PINS(lhk); }
        if constexpr (j == 41) { lhv ^= 32768u; PINS(lhv); }
        if constexpr (j == 42) { kxs = kxs + ((lhk & 32768u) ? (u32)-65536 : 65536u); PINS(kxs); }
        if constexpr (j == 44) { qs0 ^= 0x3800u; PINS(qs0); }
        if constexpr (j == 45) { qs1 ^= 0x3800u; PINS(qs1); }
      }
      GAP();
      auto vflip = [&](int jj) { if (FIRST) va[jj] += VRING + hh * 8192 + 16384 - (hh ? V0B : V0A); else va[jj] ^= 32768u; PIN(va[jj]); };
      if constexpr (j < 12) sm_ops<50 + j>(s0, s1, p, l);
      else sm_ops<j>(n0, n1, p, l);
      if constexpr (ODD) {
        if constexpr (!FIRST) {
          if constexpr (j == 7) { pw = pa + pwo; PIN(pw); }
          if constexpr (j == 12 || j == 13) { sx[j - 10] = __builtin_amdgcn_exp2f(sx[j - 10]); PIN(sx); }
          if constexpr (j == 15) { pxw[0] = cvtpk(sx[0], sx[1]); PIN(pxw[0]); }
          if constexpr (j == 17) { pxw[1] = cvtpk(sx[2], sx[3]); PIN(pxw[1]); }
          if constexpr (j == 19 || j == 21) { l[3] += sx[2 + (j - 19) / 2]; PIN(l[3]); }
        }
        if constexpr (j == 23) { kxa[0] = ka[0] + kxs; PIN(kxa[0]); }
        if constexpr (j == 25) { qa[0] = pa + qs0; PIN(qa[0]); }
        if constexpr (j == 27) { kxa[1] = ka[1] + kxs; PIN(kxa[1]); }
        if constexpr (j == 29) { qa[1] = pa + qs1; PIN(qa[1]); }
        if constexpr (j == 31 || j == 33) { ka[(j - 31) / 2] ^= 32768u; PIN(ka[(j - 31) / 2]); }
        if constexpr (j == 39 || j == 41) vflip((j - 39) / 2);
      } else {
        if constexpr (j == 12 || j == 13) { sx[j - 12] = __builtin_amdgcn_exp2f(sx[j - 12] - m12); PIN(sx); }
        if constexpr (j == 15 || j == 17) { sx[2 + (j - 15) / 2] -= m12; PIN(sx); }
        if constexpr (j == 19 || j == 21) { l[3] += sx[(j - 19) / 2]; PIN(l[3]); }
        if constexpr (j == 41) { pa ^= 2048u; PIN(pa); }
      }
      GAP();
      if constexpr (ODD && (j == 32 || j == 38 || j == 44 || j == 47)) {
        constexpr int u = j == 32 ? 0 : (j == 38 ? 1 : (j == 44 ? 2 : 3));
        sx = MFMA(kx[u], qx[u], u == 0 ? ZERO4 : sx);
        GAP();
        if constexpr (j == 38) { SETPRIO(2); GAP(); vflip(2); }
        if constexpr (j == 44) vflip(3);
        GAP();
      }
      if constexpr (j == 23) {
        if constexpr (FIRST) asm volatile("s_waitcnt vmcnt(2)\n\ts_barrier" ::: "memory");
        else if constexpr (ODD) asm volatile("s_waitcnt vmcnt(4) lgkmcnt(4)\n\ts_barrier" ::: "memory");
        else asm volatile("s_waitcnt vmcnt(4)\n\ts_barrier" ::: "memory");
        SETPRIO(3);
        GAP();
      }
    });
#pragma unroll
    for (int q = 0; q < 3; ++q) s1[q] = n1[q];
  };
  step(IC<1>{}, IC<1>{});
  STAMP(8);
#pragma unroll 1
  for (u32 t = 2; t < 16; t += 2) {
    step(IC<0>{}, IC<0>{});
    step(IC<1>{}, IC<0>{});
  }
  STAMP(11);
  SETPRIO(0);
  la16 = (pa ^ 2048u) - p12s;
  if (threadIdx.x < 2) cnts[(bid * 2 + threadIdx.x) * 32] = 0;
  {
    bf16x8 vf[8];
    const u32x4 px = *(const LAS u32x4*)(size_t)(la16 + (p12s + 3072));
#pragma unroll
    for (int i = 0; i < 4; ++i) vf[i] = vfrag(va, i, 0);
    GAP();
    sfor<50, 62>([&](auto pc) { sm_ops<decltype(pc)::v>(s0, s1, p, l); });
    GAP();
    bool bad = false;
    sfor<0, 8>([&](auto ic) {
      constexpr int i = decltype(ic)::v;
      if constexpr (i < 4) vf[i + 4] = vfrag(va, i + 4, 0);
#pragma unroll
      for (int q = 0; q < 3; ++q) o[q][i] = MFMA(vf[i], asb(p[q]), o[q][i]);
      if constexpr (i >= 6) ox[i - 6] = MFMA(vf[i], asb(px), ox[i - 6]);
      if constexpr (i < 4) { l[i] = xsum_g(l[i]); bad = bad || !(l[i] < 1.0e15f); }
      if constexpr (i == 4) {
        *(LAS float*)(size_t)(lds0 + FLG + hh * 256 + wi * 64 + ((la16 >> 2) & 60)) = l[3];
        *(LAS u32*)(size_t)(lds0 + FLG + 1024 + wid * 4) = __any(bad) ? 1u : 0u;
      }
      GAP();
    });
  }
  asm volatile("s_waitcnt lgkmcnt(0)\n\ts_barrier" ::: "memory");
  const u32 lsa = lds0 + FLG + hh * 256 + ((la16 >> 2) & 60);
  l[3] = (*(const LAS float*)(size_t)lsa + *(const LAS float*)(size_t)(lsa + 64)) + (*(const LAS float*)(size_t)(lsa + 128) + *(const LAS float*)(size_t)(lsa + 192));
  const u32x4 fl0 = *(const LAS u32x4*)(size_t)(lds0 + FLG + 1024), fl1 = *(const LAS u32x4*)(size_t)(lds0 + FLG + 1040);
  const u32 redo = __builtin_amdgcn_readfirstlane((fl0[0] | fl0[1] | fl0[2] | fl0[3]) | (fl1[0] | fl1[1] | fl1[2] | fl1[3]));
  auto epilogue = [&]() {
    PIN(la16);
    u16* ows_head = ows + ((size_t)((br * 8 + b) * 128 + h * 4) * 13) * 512 + (la16 >> 1);
#pragma unroll
    for (int q = 0; q < 3; ++q) {
      const float inv = 1.0f / l[q];
#pragma unroll
      for (int u = 0; u < 4; ++u) {
        const f32x4 a = o[q][IPOS[2 * u]] * inv, bb = o[q][IPOS[2 * u + 1]] * inv;
        const u32 a0 = cvtpk(a[0], a[1]), a1 = cvtpk(a[2], a[3]), b0 = cvtpk(bb[0], bb[1]), b1 = cvtpk(bb[2], bb[3]);
        *(u32x4*)(ows_head + (size_t)((u ^ (wi >> 1)) * 13 + wi * 3 + q) * 512) = u32x4{odd ? b0 : a0, odd ? b1 : a1, odd ? a0 : b0, odd ? a1 : b1};
      }
    }
    const float inv = 1.0f / l[3];
#pragma unroll
    for (int hf = 0; hf < 2; ++hf) {
      const f32x4 a = ox[hf] * inv;
      *(u32x2*)(ows_head + (size_t)((hf * 2 + (wi >> 1)) * 13 + 12) * 512 + (wi & 1) * 4) = u32x2{cvtpk(a[0], a[1]), cvtpk(a[2], a[3])};
    }
  };
  STAMP(12);
  epilogue();
  STAMP(13);
  if (redo) {
    float m[4];
#pragma unroll
    for (int q = 0; q < 4; ++q) {
      l[q] = 0.f;
      m[q] = -3.0e38f;
    }
#pragma unroll
    for (int q = 0; q < 3; ++q)
#pragma unroll
      for (int dt = 0; dt < 8; ++dt) o[q][dt] = ZERO4;
    ox[0] = ox[1] = ZERO4;
#pragma unroll 1
    for (int t = 0; t < 16; ++t) {
      WAIT_BAR(0);
      kv_piece(rk, t, kvl, 0); kv_piece(rk, t, kvl, 1);
      kv_piece(rv, t, kvl + VRING + 32768, 0); kv_piece(rv, t, kvl + VRING + 32768, 1);
      WAIT_BAR(0);
      safe_block<8, 0>(ka, va, qf[0], o[0], m[0], l[0]);
      safe_block<8, 0>(ka, va, qf[1], o[1], m[1], l[1]);
      safe_block<8, 0>(ka, va, qf[2], o[2], m[2], l[2]);
      bf16x8 qx[4];
      PIN(la16);
#pragma unroll
      for (int q = 0; q < 4; ++q) qx[q] = q12frag(q);
      safe_block<2, 6>(ka, va, qx, ox, m[3], l[3]);
    }
#pragma unroll
    for (int q = 0; q < 4; ++q) l[q] = xsum_g(l[q]);
    epilogue();
  }
}

constexpr u32 SL2 = 21504;
constexpr size_t WS_PART = 32u << 20, WS_CNT = 30u << 20;
#define STAMP2(i)
#define ST2ARG
#define ST2PASS
DI f32x4 bf4(u32 lo, u32 hi) { return f32x4{__uint_as_float(lo << 16), __uint_as_float(lo & 0xffff0000u), __uint_as_float(hi << 16), __uint_as_float(hi & 0xffff0000u)}; }
template <int NB, int KQ>
DI void k2_tail(f32x4 (&acc)[NB][2], rsrc_t rp, u32 pbase, u32* cnt, u32 lane, u16* __restrict__ outp ST2ARG) {
  constexpr int B0 = (NB == 7) ? 0 : 7;
  constexpr u32 PS = PART_BF16 ? 4096 : 8192;
  const u32 la16 = lane * 16, g = lane >> 4;
#pragma unroll
  for (int q = 0; q < NB; ++q)
    if (((B0 + q) & 3) != KQ) {
      if (PART_BF16) {
        const u32x4 w = {cvtpk(acc[q][0][0], acc[q][0][1]), cvtpk(acc[q][0][2], acc[q][0][3]), cvtpk(acc[q][1][0], acc[q][1][1]), cvtpk(acc[q][1][2], acc[q][1][3])};
        __builtin_amdgcn_raw_buffer_store_b128(w, rp, la16, pbase + ((B0 + q) * 4 + KQ) * PS, 16);
      } else {
#pragma unroll
        for (int e = 0; e < 2; ++e)
          __builtin_amdgcn_raw_buffer_store_b128(__builtin_bit_cast(u32x4, acc[q][e]), rp, la16, pbase + ((B0 + q) * 4 + KQ) * PS + e * 1024, 16);
      }
    }
  STAMP2(3);
  asm volatile("s_waitcnt vmcnt(0)" ::: "memory");
  STAMP2(4);
  if (lane == 0) {
    __hip_atomic_fetch_add(cnt, 1u, __ATOMIC_RELAXED, __HIP_MEMORY_SCOPE_AGENT);
    for (u32 spin = 0; spin < (1u << 22); ++spin) {
      if (__hip_atomic_load(cnt, __ATOMIC_RELAXED, __HIP_MEMORY_SCOPE_AGENT) >= 4u) break;
      __builtin_amdgcn_s_sleep(1);
    }
  }
  asm volatile("s_waitcnt vmcnt(0)" ::: "memory");
  STAMP2(5);
  u32x4 pv[2][3][2];
#pragma unroll
  for (int q = 0, n = 0; q < NB; ++q)
    if (((B0 + q) & 3) == KQ) {
#pragma unroll
      for (int s = 0; s < 3; ++s)
#pragma unroll
        for (int e = 0; e < (PART_BF16 ? 1 : 2); ++e)
          pv[n][s][e] = __builtin_amdgcn_raw_buffer_load_b128(rp, la16, pbase + ((B0 + q) * 4 + ((KQ + 1 + s) & 3)) * PS + e * 1024, 16);
      ++n;
    }
  const u32 cg = ((g & 1) << 1) | (g >> 1);
#pragma unroll
  for (int q = 0, n = 0; q < NB; ++q)
    if (((B0 + q) & 3) == KQ) {
      f32x4 r[2];
#pragma unroll
      for (int e = 0; e < 2; ++e) {
        f32x4 t[3];
#pragma unroll
        for (int s = 0; s < 3; ++s) t[s] = PART_BF16 ? bf4(pv[n][s][0][2 * e], pv[n][s][0][2 * e + 1]) : __builtin_bit_cast(f32x4, pv[n][s][e]);
        r[e] = (acc[q][e] + t[0]) + (t[1] + t[2]);
      }
      auto s0 = __builtin_amdgcn_permlane16_swap(cvtpk(r[0][0], r[0][1]), cvtpk(r[1][0], r[1][1]), false, false);
      auto s1 = __builtin_amdgcn_permlane16_swap(cvtpk(r[0][2], r[0][3]), cvtpk(r[1][2], r[1][3]), false, false);
      *(u32x4*)(outp + (size_t)((B0 + q) * 16) * 1024 + cg * 8) = u32x4{s0[0], s1[0], s0[1], s1[1]};
      ++n;
    }
  STAMP2(6);
  STAMP2(7);
}

template <int NB>
DI void k2_body(rsrc_t ro, rsrc_t rw, rsrc_t rp, u32 lds0, u32 wid, u32 lane, u32 kq, u32 rot, u32 pbase, u32* cnt, u16* __restrict__ outp ST2ARG) {
  const u32 g = lane >> 4, r16 = lane & 15, q4 = r16 >> 2, p4 = lane & 3;
  const u32 j3 = lane >> 3, j7 = lane & 7, j4 = lane >> 4;
  const u32 np = wid & 3;
  STAMP2(0);
  const u32 voffO = lane * 16, voffO8 = voffO + 8192, voffO16 = voffO + 16384;
  const u32 voffW = j3 * 1024 + (((j7 >> 1) ^ j4) * 32) + (lane & 1) * 16;
  u32 s0 = lds0, s1 = lds0 + SL2;
  constexpr u32 RM2 = ROT2 == 1 ? 3 : (ROT2 == 2 ? 7 : (ROT2 == 3 ? 15 : (ROT2 == 4 ? 1 : 0)));
  u32 jd = 0, sw = wid * 8192 + (rot & RM2) * 65536, so = wid * 1024 + (rot & RM2) * 26624;
  const u32 cw = 13312 + np * 1024, c3 = wid < 5 ? wid * 1024 + 8192 : wid * 1024 - 5120, so5 = wid < 2 ? 24576 : (1u << 24);
  u32 so5c;
  auto dop = [&](int k) {
    if (k == 0) { const u32 m = (NB == 7 ? s0 : s1) + cw; SET_M0(m, 0); }
    if (k == 1) DMA_WO(voffW, rw, sw, 0);
    if (k == 2) { const u32 m = (NB == 7 ? s0 : s1) + cw; SET_M0(m, 3968); }
    if (k == 3) DMA_WO(voffW, rw, sw, 128);
    if (k == 4) { const u32 m = s0 + wid * 1024; SET_M0(m, 0); }
    if (k == 5) DMA_O2(voffO, ro, so, 0);
    if (k == 6) { const u32 m = (wid < 5 ? s0 : s1) + c3; SET_M0(m, 0); }
    if (k == 7) DMA_O2(voffO8, ro, so, 0);
    if (k == 8) { const u32 m = s1 + wid * 1024; SET_M0(m, 3072); }
    if (k == 9) DMA_O2(voffO16, ro, so, 0);
    if (k == 10) { const u32 m = wid < 2 ? s1 + wid * 1024 + 11264 : lds0 + 7 * SL2 + wid * 1024; SET_M0(m, 0); so5c = so + so5; PINS(so5c); }
    if (k == 11) DMA_O2(voffO, ro, so5c, 0);
    if (k == 12) { ++jd; const u32 pj = (jd & ~RM2) | ((jd + rot) & RM2); sw = wid * 8192 + pj * 65536; so = wid * 1024 + pj * 26624; PINS(sw); PINS(so); PINS(jd); }
    if (k == 13) { s0 = (s0 >= lds0 + 5 * SL2) ? s0 - 5 * SL2 : s0 + 2 * SL2; PINS(s0); }
    if (k == 14) { s1 = (s1 >= lds0 + 5 * SL2) ? s1 - 5 * SL2 : s1 + 2 * SL2; PINS(s1); }
  };
#pragma unroll
  for (int jp = 0; jp < 3; ++jp)
#pragma unroll
    for (int k = 0; k < 15; ++k) { dop(k); if (!(k & 1) && k < 12) asm volatile("s_nop 0"); }
  u32 oa = lds0 + (NB == 7 ? 0 : 7168) + lane * 16;
  u32 wa = lds0 + 13312 + (np >> 1) * 4096 + (4 * g + q4) * 128 + ((((q4 >> 1) | ((g & 1) << 1)) ^ ((np & 1) * 2)) * 32) + p4 * 8;
  u32 wb = wa ^ 32u;
  f32x4 acc[NB][2];
#pragma unroll
  for (int q = 0; q < NB; ++q) acc[q][0] = acc[q][1] = ZERO4;
  WAIT_BAR(12);
  STAMP2(1);
  bf16x8 w0 = ldstr2(wa, wa + 2048), w1 = ldstr2(wb, wb + 2048), oc[3];
#pragma unroll
  for (int q = 0; q < 3; ++q) oc[q] = lds128(oa + q * 1024);
  u32 rs = 0;
  auto kstep = [&](auto oddc, auto firstc) {
    constexpr bool ODD = decltype(oddc)::v, FIRST = decltype(firstc)::v;
    bf16x8 of[NB + 3];
    bf16x4 n0l, n0h, n1l, n1h;
#pragma unroll
    for (int q = 0; q < 3; ++q) of[q] = oc[q];
    const u32 d = (rs == 6) ? (u32)(-6 * (int)SL2) : SL2;
    rs = (rs == 6) ? 0 : rs + 1;
    GAP();
    sfor<0, NB>([&](auto nc) {
      constexpr int q = decltype(nc)::v;
      constexpr int NO = NB - 2;
      constexpr int k0 = ODD ? 2 * (q - 2) : 2 * NO + 2 * q;
      acc[q][0] = MFMA(w0, of[q], acc[q][0]);
      GAP();
      if constexpr (q + 3 < NB) of[q + 3] = lds128(oa + (q + 3) * 1024);
      else of[q + 3] = lds128(oa + (q + 3 - NB) * 1024);
      if constexpr (!FIRST && k0 >= 0 && k0 < 15) dop(k0);
      GAP();
      if constexpr (q == NB - 5) { wa += d; wb += d; PIN(wa); PIN(wb); }
      if constexpr (q == NB - 4) { oa += d; PIN(oa); }
      GAP();
      acc[q][1] = MFMA(w1, of[q], acc[q][1]);
      GAP();
      if constexpr (q == NB - 4) { n0l = ldstr(wa); n0h = ldstr(wa + 2048); }
      if constexpr (q == NB - 3) { n1l = ldstr(wb); n1h = ldstr(wb + 2048); }
      if constexpr (!FIRST && k0 >= 0 && k0 + 1 < 15) dop(k0 + 1);
      GAP();
      if constexpr (ODD && q == 1) { asm volatile("s_waitcnt vmcnt(6)\n\ts_barrier" ::: "memory"); GAP(); }
    });
#pragma unroll
    for (int q = 0; q < 3; ++q) oc[q] = of[NB + q];
    w0 = __builtin_shufflevector(n0l, n0h, 0, 1, 2, 3, 4, 5, 6, 7);
    w1 = __builtin_shufflevector(n1l, n1h, 0, 1, 2, 3, 4, 5, 6, 7);
  };
  kstep(IC<0>{}, IC<1>{});
#pragma unroll 1
  for (u32 jp = 0; jp < 15; ++jp) { kstep(IC<1>{}, IC<0>{}); kstep(IC<0>{}, IC<0>{}); }
  kstep(IC<1>{}, IC<1>{});
  STAMP2(2);
  outp += (size_t)r16 * 1024 + np * 32;
  pbase += np * (PART_BF16 ? 1024 : 2048);
  if (kq == 0) k2_tail<NB, 0>(acc, rp, pbase, cnt, lane, outp ST2PASS);
  else if (kq == 1) k2_tail<NB, 1>(acc, rp, pbase, cnt, lane, outp ST2PASS);
  else if (kq == 2) k2_tail<NB, 2>(acc, rp, pbase, cnt, lane, outp ST2PASS);
  else k2_tail<NB, 3>(acc, rp, pbase, cnt, lane, outp ST2PASS);
}

__global__ __launch_bounds__(512, 2) void k2_oproj(const u16* __restrict__ ows, const u16* __restrict__ woA, const u16* __restrict__ woB, u16* __restrict__ out,
                                                   float* __restrict__ part, u32* __restrict__ cnts) {
  extern __shared__ __attribute__((aligned(16))) char smem[];
  const u32 bid = blockIdx.x, xcd = bid & 7, r = bid >> 3;
  const u32 br = xcd & 1, kq = xcd >> 1, b = r & 7, nt = r >> 3;
  const u32 lane = threadIdx.x & 63;
  const u32 wid = __builtin_amdgcn_readfirstlane(threadIdx.x >> 6);
  const u32 grp = (br * 8 + b) * 4 + nt;
  const u32 rot = ROT2 == 2 ? 2 * nt + b : (ROT2 == 3 ? 4 * nt + 2 * b : nt + b);
  const rsrc_t ro = mk_rsrc(ows + ((size_t)(br * 8 + b) * 128 + kq * 32) * 13 * 512, 32 * 13312);
  const rsrc_t rw = mk_rsrc((br ? woB : woA) + (size_t)kq * 1024 * 512 + nt * 128, 1024 * 1024 - nt * 256);
  const rsrc_t rp = mk_rsrc(part, 64 * 13 * 4 * 8192);
  u16* outp = out + (size_t)(b * 208) * 1024 + br * 512 + nt * 128;
  const u32 lds0 = (u32)(size_t)(LAS char*)smem;
  asm volatile("s_nop 4");
  if (wid < 4) k2_body<7>(ro, rw, rp, lds0, wid, lane, kq, rot, grp * (13 * 4 * 8192), cnts + (grp * 8 + wid) * 32, outp ST2PASS);
  else k2_body<6>(ro, rw, rp, lds0, wid, lane, kq, rot, grp * (13 * 4 * 8192), cnts + (grp * 8 + wid) * 32, outp ST2PASS);
}

extern "C" void kernel_launch(void* const* d_in, const int* in_sizes, int n_in, void* d_out, int out_size, void* d_ws, size_t ws_size, hipStream_t stream) {
  (void)in_sizes; (void)n_in; (void)out_size; (void)ws_size;
  u16* ows = (u16*)d_ws;
  float* part = (float*)((char*)d_ws + WS_PART);
  u32* cnts = (u32*)((char*)d_ws + WS_CNT);
  (void)hipFuncSetAttribute((const void*)k1_qattn, hipFuncAttributeMaxDynamicSharedMemorySize, 163840);
  (void)hipFuncSetAttribute((const void*)k2_oproj, hipFuncAttributeMaxDynamicSharedMemorySize, 7 * SL2 + 8192);
  k1_qattn<<<256, 512, 163840, stream>>>((const u16*)d_in[0], (const u16*)d_in[1], (const u16*)d_in[2], (const u16*)d_in[3],
                                         (const u16*)d_in[5], (const u16*)d_in[6], (const u16*)d_in[7], (const u16*)d_in[8],
                                         (const u16*)d_in[4], (const u16*)d_in[9], ows, cnts);
  k2_oproj<<<256, 512, 7 * SL2 + 8192, stream>>>(ows, (const u16*)d_in[4], (const u16*)d_in[9], (u16*)d_out, part, cnts);
}
```

```cpp
#include <hip/hip_runtime.h>
#include <hip/hip_bf16.h>

typedef unsigned short u16;
typedef unsigned int u32;
typedef __bf16 bf16x8 __attribute__((ext_vector_type(8)));
typedef __bf16 bf16x4 __attribute__((ext_vector_type(4)));
typedef __bf16 bf16x2 __attribute__((ext_vector_type(2)));
typedef float f32x4 __attribute__((ext_vector_type(4)));
typedef float f32x2 __attribute__((ext_vector_type(2)));
typedef u32 u32x4 __attribute__((ext_vector_type(4)));
#define LAS __attribute__((address_space(3)))
#define DI __device__ __forceinline__
typedef __amdgpu_buffer_rsrc_t rsrc_t;

#define MFMA(a, b, c) __builtin_amdgcn_mfma_f32_16x16x32_bf16((a), (b), (c), 0, 0, 0)
#define WAIT_BAR(N) asm volatile("s_waitcnt vmcnt(" #N ") lgkmcnt(0)\n\ts_barrier" ::: "memory")

DI rsrc_t mk_rsrc(const void* p, u32 bytes) { return __builtin_amdgcn_make_buffer_rsrc((void*)p, (short)0, (int)bytes, 0x00020000); }
DI void dma_nt(u32 lds, u32 voff, rsrc_t rs, u32 soff) {
  asm volatile("s_mov_b32 m0, %0\n\ts_nop 0\n\tbuffer_load_dwordx4 %1, %2, %3 offen nt lds" ::"s"(lds), "v"(voff), "s"(rs), "s"(soff));
}
DI void dma_df(u32 lds, u32 voff, rsrc_t rs, u32 soff) {
  asm volatile("s_mov_b32 m0, %0\n\ts_nop 0\n\tbuffer_load_dwordx4 %1, %2, %3 offen lds" ::"s"(lds), "v"(voff), "s"(rs), "s"(soff));
}
DI void dma_sc1(u32 lds, u32 voff, rsrc_t rs, u32 soff) {
  asm volatile("s_mov_b32 m0, %0\n\ts_nop 0\n\tbuffer_load_dwordx4 %1, %2, %3 offen sc1 lds" ::"s"(lds), "v"(voff), "s"(rs), "s"(soff));
}
#define ACQ 1
#define NPUB 4
#define XPT 4
#define YPT 5
#define POL_XW 1
#define POL_O2 0
#define POL_WO 1
DI void dma_xw(u32 lds, u32 voff, rsrc_t rs, u32 soff) { if (POL_XW) dma_nt(lds, voff, rs, soff); else dma_df(lds, voff, rs, soff); }
DI void dma_o2(u32 lds, u32 voff, rsrc_t rs, u32 soff) { if (POL_O2) dma_nt(lds, voff, rs, soff); else dma_df(lds, voff, rs, soff); }
DI void dma_wo(u32 lds, u32 voff, rsrc_t rs, u32 soff) { if (POL_WO) dma_nt(lds, voff, rs, soff); else dma_df(lds, voff, rs, soff); }

DI bf16x8 lds128(u32 a) { return *(const LAS bf16x8*)(size_t)a; }
DI bf16x4 ldstr(u32 a) { return __builtin_amdgcn_ds_read_tr16_b64_v4bf16((LAS bf16x4*)(size_t)a); }
DI bf16x8 ldstr2(u32 a0, u32 a1) { bf16x4 lo = ldstr(a0), hi = ldstr(a1); return __builtin_shufflevector(lo, hi, 0, 1, 2, 3, 4, 5, 6, 7); }
DI u32 cvtpk(float lo, float hi) { f32x2 v = {lo, hi}; return __builtin_bit_cast(u32, __builtin_convertvector(v, bf16x2)); }
DI float xsum_g(float v) {
  auto a = __builtin_amdgcn_permlane16_swap(__float_as_uint(v), __float_as_uint(v), false, false);
  v = __uint_as_float(a[0]) + __uint_as_float(a[1]);
  auto b = __builtin_amdgcn_permlane32_swap(__float_as_uint(v), __float_as_uint(v), false, false);
  return __uint_as_float(b[0]) + __uint_as_float(b[1]);
}
DI float xmax_g(float v) {
  auto a = __builtin_amdgcn_permlane16_swap(__float_as_uint(v), __float_as_uint(v), false, false);
  v = fmaxf(__uint_as_float(a[0]), __uint_as_float(a[1]));
  auto b = __builtin_amdgcn_permlane32_swap(__float_as_uint(v), __float_as_uint(v), false, false);
  return fmaxf(__uint_as_float(b[0]), __uint_as_float(b[1]));
}

constexpr float QSCALE = 0.08838834764831845f * 1.4426950408889634f;
#define GAP() __builtin_amdgcn_sched_barrier(0)
#define PIN(x) asm volatile("" : "+v"(x))
#define ZERO4 (f32x4{0.f, 0.f, 0.f, 0.f})
template <int N> struct IC { static constexpr int v = N; };
template <int B, int E, class F> DI void sfor(F&& f) { if constexpr (B < E) { f(IC<B>{}); sfor<B + 1, E>(f); } }

#define STAMP(i)
#define ROT 3
#define ROT2 1
#define WOPF 0
#define PART_BF16 1
#define PRIO 0
#define KVIDX (b * 32 + h)
#define PD 4
constexpr u32 VRING = 65536, XBUF = 65536, XSZ = 26624, K0A = 131072, V0A = 147456, V0B = 122880, Q12 = 155648, P12 = 131072, FLG = 139264;
typedef u32 u32x2 __attribute__((ext_vector_type(2)));

DI float max8(const f32x4& a, const f32x4& b) { return fmaxf(fmaxf(fmaxf(a[0], a[1]), fmaxf(a[2], a[3])), fmaxf(fmaxf(b[0], b[1]), fmaxf(b[2], b[3]))); }
DI bf16x8 asb(const u32x4& w) { return __builtin_bit_cast(bf16x8, w); }
DI bf16x8 kfrag(const u32 (&ka)[2], int f, u32 off) { return lds128(ka[f & 1] + (((f >> 1) & 1) * 4096 + (f >> 2) * 2048 + off)); }
DI bf16x8 vfrag(const u32 (&va)[4], int i, u32 off) { const u32 a = va[((i >> 1) + 1) & 3] + ((i & 1) * 4096 + off); return ldstr2(a, a + 2048); }

template <int NT, int I0>
DI void safe_block(u32 (&ka)[2], u32 (&va)[4], const bf16x8 (&qf)[4], f32x4 (&o)[NT], float& m, float& l) {
  f32x4 s[2];
#pragma unroll
  for (int f = 0; f < 8; ++f) { s[f >> 2] = MFMA(kfrag(ka, f, 0), qf[f & 3], (f & 3) ? s[f >> 2] : ZERO4); GAP(); }
  const float mx = fmaxf(xmax_g(max8(s[0], s[1])), m);
  const float al = __builtin_amdgcn_exp2f(m - mx);
  m = mx;
  float e[8];
#pragma unroll
  for (int i = 0; i < 8; ++i) e[i] = __builtin_amdgcn_exp2f(s[i >> 2][i & 3] - mx);
  l = l * al + (((e[0] + e[1]) + (e[2] + e[3])) + ((e[4] + e[5]) + (e[6] + e[7])));
  u32x4 w = {cvtpk(e[0], e[1]), cvtpk(e[2], e[3]), cvtpk(e[4], e[5]), cvtpk(e[6], e[7])};
  GAP();
#pragma unroll
  for (int i = 0; i < NT; ++i) { o[i] *= al; PIN(o[i]); o[i] = MFMA(vfrag(va, I0 + i, 0), asb(w), o[i]); PIN(o[i]); GAP(); }
}

constexpr int EP(int k) { return 14 + 2 * k; }
constexpr int CW(int q, int w) { return w < 2 ? 43 + 2 * q : (q == 0 ? 49 + 2 * (w - 2) : (q == 1 ? 53 + 2 * (w - 2) : 59 + 2 * (w - 2))); }

template <int POS>
DI void sm_ops(f32x4 (&s0)[3], f32x4 (&s1)[3], u32x4 (&p)[3], float (&l)[4]) {
  sfor<0, 24>([&](auto kc) {
    constexpr int k = decltype(kc)::v, q = (k % 12) / 4, r = k & 3;
    if constexpr (EP(k) + 1 == POS) { l[q] += (k < 12) ? s0[q][r] : s1[q][r]; PIN(l[q]); }
    if constexpr (EP(k) == POS) {
      if constexpr (k < 12) { s0[q][r] = __builtin_amdgcn_exp2f(s0[q][r]); PIN(s0[q]); }
      else { s1[q][r] = __builtin_amdgcn_exp2f(s1[q][r]); PIN(s1[q]); }
    }
  });
  sfor<0, 12>([&](auto qc) {
    constexpr int q = decltype(qc)::v / 4, w = decltype(qc)::v & 3;
    if constexpr (CW(q, w) == POS) {
      if constexpr (w < 2) p[q][w] = cvtpk(s0[q][2 * w], s0[q][2 * w + 1]); else p[q][w] = cvtpk(s1[q][2 * w - 4], s1[q][2 * w - 3]);
      PIN(p[q]);
    }
  });
}
#define SET_M0(base, imm) asm volatile("s_add_u32 m0, %0, %1" ::"s"(base), "i"(imm) : "scc")
#define DMA_LD(voff, rs, soff, imm) asm volatile("buffer_load_dwordx4 %0, %1, %2 offen offset:%3 nt lds" ::"v"(voff), "s"(rs), "s"(soff), "i"(imm))
#define DMA_XW DMA_LD
#define DMA_DF(voff, rs, soff, imm) asm volatile("buffer_load_dwordx4 %0, %1, %2 offen offset:%3 lds" ::"v"(voff), "s"(rs), "s"(soff), "i"(imm))
#define DMA_WO DMA_LD
#define DMA_O2 DMA_DF
#define PINS(x) asm volatile("" : "+s"(x))
#define SETPRIO(n)

constexpr u32 SL2 = 21504, XO7 = 7 * SL2;
constexpr size_t WS_PART = 32u << 20, WS_FLG = 30u << 20;
#define STAMP2(i)
#define ST2ARG
#define ST2PASS
DI f32x4 bf4(u32 lo, u32 hi) { return f32x4{__uint_as_float(lo << 16), __uint_as_float(lo & 0xffff0000u), __uint_as_float(hi << 16), __uint_as_float(hi & 0xffff0000u)}; }
DI u32 flag_ld(rsrc_t rf, u32 off) { return __builtin_amdgcn_raw_buffer_load_b32(rf, off, 0, 16); }
DI void flag_st(rsrc_t rf, u32 off, u32 v) { __builtin_amdgcn_raw_buffer_store_b32(v, rf, off, 0, 16); }
DI bool flag_pass(rsrc_t rf, u32 ld, u32 st, u32& nv, u32 nonce, u32 lane) {
  const u32 v = flag_ld(rf, ld);
  const bool chg = lane >= 3 && lane < 6 && v != nv;
  if (chg) { nv = v; flag_st(rf, st, v); }
  return __all(lane >= 3 || v == nonce);
}
DI void flag_wait(rsrc_t rf, u32 ld, u32 st, u32& nv, u32 nonce, u32 lane) {
  for (u32 spin = 0; spin < (1u << 21); ++spin) {
    if (flag_pass(rf, ld, st, nv, nonce, lane)) break;
    __builtin_amdgcn_s_sleep(1);
  }
}
struct P2 { rsrc_t ro, rw, rp, rf; u32 wid, lane, kq, j, b, pbase, nonce, ldA, stA, nvA, ldB, stB, nvB; u16* outp; };

template <int NB, int KQ>
DI void p2_tail(f32x4 (&acc)[NB][2], P2& c ST2ARG) {
  constexpr int B0 = (NB == 7) ? 0 : 7;
  constexpr u32 PS = PART_BF16 ? 4096 : 8192;
  const u32 lane = c.lane, la16 = lane * 16, g = lane >> 4;
#pragma unroll
  for (int q = 0; q < NB; ++q)
    if (((B0 + q) & 3) != KQ) {
      if (PART_BF16) {
        const u32x4 w = {cvtpk(acc[q][0][0], acc[q][0][1]), cvtpk(acc[q][0][2], acc[q][0][3]), cvtpk(acc[q][1][0], acc[q][1][1]), cvtpk(acc[q][1][2], acc[q][1][3])};
        __builtin_amdgcn_raw_buffer_store_b128(w, c.rp, la16, c.pbase + ((B0 + q) * 4 + KQ) * PS, 16);
      } else {
#pragma unroll
        for (int e = 0; e < 2; ++e)
          __builtin_amdgcn_raw_buffer_store_b128(__builtin_bit_cast(u32x4, acc[q][e]), c.rp, la16, c.pbase + ((B0 + q) * 4 + KQ) * PS + e * 1024, 16);
      }
    }
  STAMP2(3);
  asm volatile("s_waitcnt vmcnt(0)" ::: "memory");
  STAMP2(4);
  if (lane >= 3 && lane < 6) flag_st(c.rf, c.stB, c.nvB);
  if (c.wid == 0) flag_pass(c.rf, c.ldA, c.stA, c.nvA, c.nonce, lane);
  flag_wait(c.rf, c.ldB, c.stB, c.nvB, c.nonce, lane);
  asm volatile("" ::: "memory");
  STAMP2(5);
  u32x4 pv[2][3][2];
#pragma unroll
  for (int q = 0, n = 0; q < NB; ++q)
    if (((B0 + q) & 3) == KQ) {
#pragma unroll
      for (int s = 0; s < 3; ++s)
#pragma unroll
        for (int e = 0; e < (PART_BF16 ? 1 : 2); ++e)
          pv[n][s][e] = __builtin_amdgcn_raw_buffer_load_b128(c.rp, la16, c.pbase + ((B0 + q) * 4 + ((KQ + 1 + s) & 3)) * PS + e * 1024, 16);
      ++n;
    }
  flag_pass(c.rf, c.ldB, c.stB, c.nvB, c.nonce, lane);
  const u32 cg = ((g & 1) << 1) | (g >> 1);
#pragma unroll
  for (int q = 0, n = 0; q < NB; ++q)
    if (((B0 + q) & 3) == KQ) {
      f32x4 r[2];
#pragma unroll
      for (int e = 0; e < 2; ++e) {
        f32x4 t[3];
#pragma unroll
        for (int s = 0; s < 3; ++s) t[s] = PART_BF16 ? bf4(pv[n][s][0][2 * e], pv[n][s][0][2 * e + 1]) : __builtin_bit_cast(f32x4, pv[n][s][e]);
        r[e] = (acc[q][e] + t[0]) + (t[1] + t[2]);
      }
      auto s0 = __builtin_amdgcn_permlane16_swap(cvtpk(r[0][0], r[0][1]), cvtpk(r[1][0], r[1][1]), false, false);
      auto s1 = __builtin_amdgcn_permlane16_swap(cvtpk(r[0][2], r[0][3]), cvtpk(r[1][2], r[1][3]), false, false);
      *(u32x4*)(c.outp + (size_t)((B0 + q) * 16) * 1024 + cg * 8) = u32x4{s0[0], s1[0], s0[1], s1[1]};
      ++n;
    }
  STAMP2(6);
  STAMP2(7);
}

template <int NB>
DI void p2_body(P2& c ST2ARG) {
  const u32 lane = c.lane, wid = c.wid;
  const u32 g = lane >> 4, r16 = lane & 15, q4 = r16 >> 2, p4 = lane & 3;
  const u32 j3 = lane >> 3, j7 = lane & 7, j4 = lane >> 4;
  const u32 np = wid & 3;
  const u32 voffO = lane * 16, voffO8 = voffO + 8192, voffO16 = voffO + 16384;
  const u32 voffW = j3 * 1024 + (((j7 >> 1) ^ j4) * 32) + (lane & 1) * 16;
  u32 s0 = SL2, s1 = 2 * SL2, jd = 4;
  auto pair_of = [&](u32 q) { return q < 16 ? (((c.j + (q >> 2)) & 3) << 2) | ((q + c.b) & 3) : 64u; };
  u32 sw = wid * 8192 + pair_of(4) * 65536, so = wid * 1024 + pair_of(4) * 26624;
  const u32 cw = 13312 + np * 1024, c3 = wid < 5 ? wid * 1024 + 8192 : wid * 1024 - 5120;
  auto dop = [&](int k) {
    if (k == 0) { const u32 m = (NB == 7 ? s0 : s1) + cw; SET_M0(m, 0); }
    if (k == 1) DMA_WO(voffW, c.rw, sw, 0);
    if (k == 2) { const u32 m = (NB == 7 ? s0 : s1) + cw; SET_M0(m, 3968); }
    if (k == 3) DMA_WO(voffW, c.rw, sw, 128);
    if (k == 4) { const u32 m = s0 + wid * 1024; SET_M0(m, 0); }
    if (k == 5) DMA_O2(voffO, c.ro, so, 0);
    if (k == 6) { const u32 m = (wid < 5 ? s0 : s1) + c3; SET_M0(m, 0); }
    if (k == 7) DMA_O2(voffO8, c.ro, so, 0);
    if (k == 8) { const u32 m = s1 + wid * 1024; SET_M0(m, 3072); }
    if (k == 9) DMA_O2(voffO16, c.ro, so, 0);
    if (k == 10) { if (NB == 7 && wid < 2) { const u32 m = s1 + wid * 1024 + 11264, so5 = so + 24576; dma_df(m, voffO, c.ro, so5); } }
    if (k == 11) { ++jd; const u32 pj = pair_of(jd); sw = wid * 8192 + pj * 65536; so = wid * 1024 + pj * 26624; PINS(sw); PINS(so); PINS(jd); }
    if (k == 12) { s0 = (s0 >= 5 * SL2) ? s0 - 5 * SL2 : s0 + 2 * SL2; PINS(s0); }
    if (k == 13) { s1 = (s1 >= 5 * SL2) ? s1 - 5 * SL2 : s1 + 2 * SL2; PINS(s1); }
  };
  u32 oa = (NB == 7 ? 0 : 7168) + lane * 16;
  u32 wa = 13312 + (np >> 1) * 4096 + (4 * g + q4) * 128 + ((((q4 >> 1) | ((g & 1) << 1)) ^ ((np & 1) * 2)) * 32) + p4 * 8;
  u32 wb = wa ^ 32u;
  f32x4 acc[NB][2];
#pragma unroll
  for (int q = 0; q < NB; ++q) acc[q][0] = acc[q][1] = ZERO4;
  bf16x8 w0 = ldstr2(wa, wa + 2048), w1 = ldstr2(wb, wb + 2048), oc[3];
#pragma unroll
  for (int q = 0; q < 3; ++q) oc[q] = lds128(oa + q * 1024);
  u32 ks = 0, rs = 0;
  u32 pa_ = wid * SL2 + lane * 16, ps_ = (pair_of(wid >> 1) * 2 + (wid & 1)) * 13312;
  auto kstep = [&](auto modec) {
    constexpr int MODE = decltype(modec)::v;
    bf16x8 of[NB + 3];
    bf16x4 n0l, n0h, n1l, n1h;
    u32x4 pub[NPUB];
#pragma unroll
    for (int q = 0; q < 3; ++q) of[q] = oc[q];
    const u32 d = (rs == 6) ? (u32)(-6 * (int)SL2) : SL2;
    const u32 d2 = ks == 6 ? SL2 : (ks == 7 ? (u32)(-6 * (int)SL2) : d);
    rs = (rs == 6) ? 0 : rs + 1;
    ++ks;
    GAP();
    sfor<0, NB>([&](auto nc) {
      constexpr int q = decltype(nc)::v;
      constexpr int NO = NB - 2;
      constexpr int k0 = MODE == 1 ? 2 * (q - 2) : (MODE == 2 ? 2 * NO + 2 * q : -1);
      acc[q][0] = MFMA(w0, of[q], acc[q][0]);
      GAP();
      if constexpr (q + 3 < NB) of[q + 3] = lds128(oa + (q + 3) * 1024);
      else of[q + 3] = lds128(oa + (q + 3 - NB) * 1024);
      if constexpr (k0 >= 0 && k0 < 14) dop(k0);
      if constexpr (MODE == 4 && q >= 2 && q < 2 + NPUB) { if (ks * NPUB - NPUB + (q - 2) < 13) __builtin_amdgcn_raw_buffer_store_b128(pub[q - 2], c.ro, voffO, ps_ + (q - 2) * 1024, 16); }
      GAP();
      if constexpr (q == NB - 5) { wa += d; wb += d; PIN(wa); PIN(wb); }
      if constexpr (q == NB - 4) { oa += d2; PIN(oa); }
      GAP();
      acc[q][1] = MFMA(w1, of[q], acc[q][1]);
      GAP();
      if constexpr (MODE == 4 && q < 2) { sfor<0, NPUB>([&](auto ic) { constexpr int i = decltype(ic)::v; if constexpr ((i & 1) == q) pub[i] = *(const LAS u32x4*)(size_t)(pa_ + i * 1024); }); }
      if constexpr (q == NB - 4) { n0l = ldstr(wa); n0h = ldstr(wa + 2048); }
      if constexpr (q == NB - 3) { n1l = ldstr(wb); n1h = ldstr(wb + 2048); }
      if constexpr (k0 >= 0 && k0 + 1 < 14) dop(k0 + 1);
      GAP();
      if constexpr ((MODE == 1 || MODE == 3) && q == 1) { asm volatile("s_waitcnt vmcnt(5)\n\ts_barrier" ::: "memory"); GAP(); }
    });
#pragma unroll
    for (int q = 0; q < 3; ++q) oc[q] = of[NB + q];
    w0 = __builtin_shufflevector(n0l, n0h, 0, 1, 2, 3, 4, 5, 6, 7);
    w1 = __builtin_shufflevector(n1l, n1h, 0, 1, 2, 3, 4, 5, 6, 7);
    if constexpr (MODE == 4) { pa_ += NPUB * 1024; ps_ += NPUB * 1024; }
  };
  STAMP2(1);
  kstep(IC<4>{});
  asm volatile("s_waitcnt vmcnt(%0)\n\ts_barrier" ::"i"(NPUB) : "memory");
#pragma unroll 1
  for (u32 i = 1; i < 7; ++i) {
    if (i * NPUB < 13) kstep(IC<4>{}); else kstep(IC<0>{});
    if (i == XPT) {
      asm volatile("s_waitcnt vmcnt(0)\n\ts_barrier" ::: "memory");
      if (wid == 0 && lane >= 3 && lane < 6) flag_st(c.rf, c.stA, c.nvA);
      dma_nt(13312 + (wid >> 2) * 4096 + np * 1024, voffW, c.rw, (pair_of(3) * 64 + 32 + np * 8) * 1024 + (wid >> 2) * 128);
    }
    if (i == YPT) {
      if (wid == 0) {
        flag_wait(c.rf, c.ldA, c.stA, c.nvA, c.nonce, lane);
        if (ACQ) __builtin_amdgcn_fence(__ATOMIC_ACQUIRE, "agent");
      }
      asm volatile("s_waitcnt vmcnt(0)\n\ts_barrier" ::: "memory");
      STAMP2(0);
#pragma unroll
      for (int jp = 0; jp < 2; ++jp)
#pragma unroll
        for (int k = 0; k < 14; ++k) { dop(k); if (!(k & 1) && k < 10) asm volatile("s_nop 0"); }
    }
  }
#pragma unroll 1
  for (u32 jp = 3; jp < 15; ++jp) { kstep(IC<1>{}); kstep(IC<2>{}); }
  kstep(IC<3>{});
  STAMP2(2);
  c.outp += (size_t)r16 * 1024 + np * 32;
  c.pbase += np * (PART_BF16 ? 1024 : 2048);
  if (c.kq == 0) p2_tail<NB, 0>(acc, c ST2PASS);
  else if (c.kq == 1) p2_tail<NB, 1>(acc, c ST2PASS);
  else if (c.kq == 2) p2_tail<NB, 2>(acc, c ST2PASS);
  else p2_tail<NB, 3>(acc, c ST2PASS);
}

__global__ __launch_bounds__(512, 2) void k1_qattn(const u16* __restrict__ xA, const u16* __restrict__ kA, const u16* __restrict__ vA, const u16* __restrict__ wqA,
                                                   const u16* __restrict__ xB, const u16* __restrict__ kB, const u16* __restrict__ vB, const u16* __restrict__ wqB,
                                                   const u16* __restrict__ woA, const u16* __restrict__ woB, u16* __restrict__ ows, u16* __restrict__ out) {
  extern __shared__ __attribute__((aligned(16))) char smem[];
  const u32 bid = blockIdx.x, xcd = bid & 7, rr = bid >> 3;
  const u32 br = xcd & 1, b = rr & 7, hpl = rr >> 3, hp = (xcd >> 1) * 4 + hpl;
  const u32 lane = threadIdx.x & 63;
  const u32 wid = __builtin_amdgcn_readfirstlane(threadIdx.x >> 6);
  const u32 hh = wid >> 2, wi = wid & 3, h = hp * 2 + hh;
  constexpr u32 RM = ROT == 3 ? 3 : (ROT == 4 ? 1 : 7);
  const u32 c0 = ROT == 1 ? (b + 2 * hpl) & 7 : (ROT == 2 ? ((b ^ hpl) & 1) * 4 : (ROT == 3 ? (b + hpl) & 3 : (ROT == 4 ? (b + hpl) & 1 : 0)));
  auto chunk_of = [&](u32 c) { return (c & ~RM) | ((c + c0) & RM); };
  const u32 lds0 = (u32)(size_t)(LAS char*)smem;
  const rsrc_t rx = mk_rsrc((br ? xB : xA) + (size_t)b * 208 * 512, 208 * 512 * 2);
  const rsrc_t rw = mk_rsrc(br ? wqB : wqA, 512 * 4096 * 2);
  const rsrc_t rk = mk_rsrc((br ? kB : kA) + (size_t)(KVIDX) * 512 * 128, 512 * 128 * 2);
  const rsrc_t rv = mk_rsrc((br ? vB : vA) + (size_t)(KVIDX) * 512 * 128, 512 * 128 * 2);
  STAMP(0);
  const rsrc_t rf = mk_rsrc((char*)ows + WS_FLG, 256 * 512);
  u32 inbox;
  asm volatile("s_nop 4\n\tbuffer_load_dword %0, %1, %2, 0 offen sc1" : "=v"(inbox) : "v"(bid * 512 + (lane < 28 ? (lane < 4 ? (lane ? 124 + lane * 4 : 0) : 240 + lane * 4) : 0)), "s"(rf));

  const u32 g = lane >> 4, r16 = lane & 15, q4 = r16 >> 2, p4 = lane & 3;
  const u32 j3 = lane >> 3, j7 = lane & 7, j4 = lane >> 4;
  const u32 voffX = j3 * 1024 + ((j7 ^ ((wid & 1) * 4 + j4)) * 16);
  const u32 voffW = j3 * 8192 + ((j7 ^ ((j4 & 1) | ((wid & 1) << 2))) * 16);
  const u32 voff = j3 * 256 + (((j7 >> 1) ^ j4) * 32) + (lane & 1) * 16;
  const u32 kvl = lds0 + hh * 8192 + wi * 1024;
  const u32 kvs = wi * 2048;
  auto kv_piece = [&](rsrc_t rs, u32 step, u32 lb, int hf) { dma_nt(lb + hf * 4096, voff, rs, step * 8192 + kvs + hf * 128); };
  const u32 swb = wi * 65536 + h * 256;
  u32 jw = 0, jx = 0, sw = c0 * 524288 + swb, lw = kvl + 16384;
  u32 sx_ = wid * 8192 + c0 * 128, lx = lds0 + XBUF + wid * 1024, lx3 = wid < 2 ? lx + 24576 : lds0 + Q12 + wid * 1024;
  const u32 dx3 = wid < 2 ? XSZ : 0;
  u32 voffXi[4];
#pragma unroll
  for (int i = 0; i < 4; ++i) voffXi[i] = voffX + i * 65536;
  auto w_m0 = [&](int hf) { SET_M0(lw, hf * 3968); };
  auto w_ld = [&](int hf) { DMA_XW(voffW, rw, sw, hf * 128); };
  auto w_adv = [&](int ph) { if (ph == 0) { ++jw; sw = chunk_of(jw >> 1) * 524288 + (jw & 1) * 262144 + swb; PINS(sw); PINS(jw); } else { lw = (lw + 16384) & 0xffffu; PINS(lw); } };
  auto x_m0 = [&](int i) { if (i < 3) SET_M0(lx, i * 8192); else SET_M0(lx3, 0); };
  auto x_ld = [&](int i) { DMA_XW(voffXi[i], rx, sx_, 0); };
  auto x_adv = [&](u32 c) {
    ++jx; sx_ = wid * 8192 + chunk_of(jx) * 128; PINS(jx);
    lx += (c & 1) ? (u32)-XSZ : XSZ; lx3 += (c & 1) ? (u32)-dx3 : dx3;
    PINS(sx_); PINS(lx); PINS(lx3);
  };
  auto nop1 = [&]() { asm volatile("s_nop 0"); };
  const u32 v0l = lds0 + (hh ? V0B : V0A) + wi * 1024;
  w_m0(0); nop1(); w_ld(0); w_m0(1); nop1(); w_ld(1); w_adv(0); w_adv(1);
#pragma unroll
  for (int i = 0; i < 4; ++i) { x_m0(i); nop1(); x_ld(i); }
  x_adv(0);
  w_m0(0); nop1(); w_ld(0); w_m0(1); nop1(); w_ld(1); w_adv(0); w_adv(1);

  u32 xa[3], wa[4];
  xa[0] = lds0 + XBUF + wi * 6144 + r16 * 128 + ((g ^ (r16 >> 1)) * 16);
  xa[1] = xa[0] ^ 64u;
  xa[2] = xa[0] + (4 - wi) * 6144;
  wa[0] = lds0 + 16384 + hh * 8192 + (8 * g + q4) * 128 + ((((2 * (p4 >> 1)) | (wi & 1) | ((wi >> 1) << 2)) ^ ((q4 >> 1) | ((g & 1) << 2))) * 16) + (p4 & 1) * 8;
#pragma unroll
  for (int j = 1; j < 4; ++j) wa[j] = wa[0] ^ (u32)(((j & 1) << 4) | ((j >> 1) << 6));
  f32x4 o[3][8], ox[2];
  auto wread = [&](int n, int hi) { return ldstr(wa[(((n & 7) >> 1) + 1) & 3] + ((n & 1) * 4096 + hi * 512)); };
  bf16x8 xc[4];
  bf16x4 wcl[3], wch[3];
  auto qchunk = [&](auto kindc, u32 c) {
    constexpr int KIND = decltype(kindc)::v;
    bf16x8 xf[2][4];
    bf16x4 wl[19], wh[19];
    u32 x13;
#pragma unroll
    for (int q = 0; q < 4; ++q) xf[0][q] = xc[q];
#pragma unroll
    for (int q = 0; q < 3; ++q) { wl[q] = wcl[q]; wh[q] = wch[q]; }
    const u32 dw = (c & 1) ? (u32)-49152 : 16384u, dx = (c & 1) ? (u32)-XSZ : XSZ;
    GAP();
    sfor<0, 16>([&](auto nc) {
      constexpr int n = decltype(nc)::v, i = n & 7;
      constexpr bool RD = (n + 3 < 16 || KIND != 4);
      const bf16x8 wf = __builtin_shufflevector(wl[n], wh[n], 0, 1, 2, 3, 4, 5, 6, 7);
      auto dma_op = [&](auto kc) {
        constexpr int k = decltype(kc)::v;
        constexpr bool EV = n >= 4 && n < 12;
        if constexpr (KIND == 0 && n < 4) {
          constexpr int z = k - 16;
          if constexpr (z == 0) w_m0(0); if constexpr (z == 1) { w_ld(0); w_m0(1); } if constexpr (z == 2) { w_ld(1); w_adv(0); w_adv(1); x_m0(0); }
          if constexpr (z >= 3 && z < 6) { x_ld(z - 3); x_m0(z - 2); } if constexpr (z == 6) { x_ld(3); x_adv(1); }
        } else if constexpr (n >= 4) {
          constexpr bool W = EV ? KIND <= 3 : KIND <= 2;
          if constexpr (W) {
            if constexpr (k == 0) w_m0(0); if constexpr (k == 1) w_ld(0); if constexpr (k == 2) w_m0(1); if constexpr (k == 3) w_ld(1);
            if constexpr (k == 4) w_adv(0); if constexpr (k == 5) w_adv(1);
          }
          if constexpr (EV && KIND <= 2) {
            if constexpr (k >= 6 && k < 14) { if constexpr (k & 1) x_ld((k - 6) / 2); else x_m0((k - 6) / 2); }
            if constexpr (k == 14) x_adv(c);
          }
          if constexpr (!EV && KIND == 0) {
            if constexpr (k == 6) { kv_piece(rk, 0, kvl + K0A, 0); kv_piece(rk, 0, kvl + K0A, 1); }
            if constexpr (k == 7) { kv_piece(rv, 0, v0l, 0); kv_piece(rv, 0, v0l, 1); }
          }
          if constexpr (EV && KIND == 3 && (k == 6 || k == 7)) kv_piece(rv, 1, kvl + VRING, k & 1);
          if constexpr (!EV && KIND == 3 && k < 2) kv_piece(rk, 1, kvl + 16384, k & 1);
          if constexpr (EV && KIND == 4 && k < 2) kv_piece(rk, 2, kvl + 32768, k & 1);
          if constexpr (EV && KIND == 4 && k >= 2 && k < 6) kv_piece(rv, k / 2 + 1, kvl + VRING + (k / 2) * 16384, k & 1);
          if constexpr (!EV && KIND == 4 && k < 2) kv_piece(rk, 3, kvl + 49152, k & 1);
        }
      };
      constexpr int kb = n < 4 ? 16 + 2 * n : 2 * ((n - 4) & 7);
      o[0][i] = MFMA(wf, xf[n >> 3][0], (KIND == 0 && n < 8) ? ZERO4 : o[0][i]);
      GAP();
      if constexpr (RD) { wl[n + 3] = wread(n + 3, 0); wh[n + 3] = wread(n + 3, 1); }
      GAP();
      o[1][i] = MFMA(wf, xf[n >> 3][1], (KIND == 0 && n < 8) ? ZERO4 : o[1][i]);
      GAP();
      if constexpr (n < 3) xf[1][n] = lds128(xa[1] + n * 2048);
      if constexpr (KIND != 4 && n >= 12 && n < 15) xc[n - 12] = lds128(xa[0] + (n - 12) * 2048);
      dma_op(IC<kb>{});
      GAP();
      if constexpr (n == 0) { x13 = xa[2] ^ 64u; PIN(x13); }
      if constexpr (n == 4 || (n == 12 && KIND != 4)) { wa[0] += (n == 4 ? dw : 16384u); wa[1] += (n == 4 ? dw : 16384u); PIN(wa[0]); PIN(wa[1]); }
      if constexpr (n == 6 && KIND != 4) { xa[0] += dx; xa[1] += dx; PIN(xa[0]); PIN(xa[1]); }
      GAP();
      o[2][i] = MFMA(wf, xf[n >> 3][2], (KIND == 0 && n < 8) ? ZERO4 : o[2][i]);
      GAP();
      if constexpr (n == 1) xf[1][3] = lds128(x13);
      if constexpr (KIND != 4 && n == 15) xc[3] = lds128(xa[2]);
      dma_op(IC<kb + 1>{});
      GAP();
      if constexpr (n == 4 || (n == 12 && KIND != 4)) { wa[2] += (n == 4 ? dw : 16384u); wa[3] += (n == 4 ? dw : 16384u); PIN(wa[2]); PIN(wa[3]); }
      if constexpr (n == 6 && KIND != 4) { xa[2] += dx; PIN(xa[2]); }
      GAP();
      if constexpr (i >= 6) { ox[i - 6] = MFMA(wf, xf[n >> 3][3], (KIND == 0 && n < 8) ? ZERO4 : ox[i - 6]); GAP(); }
      if constexpr (n == 3 || n == 11) {
        if constexpr (KIND == 4 && n == 11) asm volatile("s_barrier" ::: "memory");
        else if constexpr (KIND == 1) asm volatile("s_waitcnt vmcnt(10) lgkmcnt(2)\n\ts_barrier" ::: "memory");
        else if constexpr ((KIND == 3 && n == 11) || KIND == 4) asm volatile("s_waitcnt vmcnt(4) lgkmcnt(2)\n\ts_barrier" ::: "memory");
        else asm volatile("s_waitcnt vmcnt(6) lgkmcnt(2)\n\ts_barrier" ::: "memory");
        GAP();
      }
    });
#pragma unroll
    for (int q = 0; q < 3; ++q) { wcl[q] = wl[16 + q]; wch[q] = wh[16 + q]; }
  };
  STAMP(1);
  asm volatile("s_waitcnt vmcnt(2) lgkmcnt(0)\n\ts_barrier" : "+v"(inbox)::"memory");
  STAMP(2);
  u32 nonce = __builtin_amdgcn_readfirstlane(inbox) + 1;
  if (wid == 0) {
    while (__any(inbox == nonce)) ++nonce;
    if (lane == 0) flag_st(rf, bid * 512, nonce);
  }
#pragma unroll
  for (int q = 0; q < 3; ++q) { wcl[q] = wread(q, 0); wch[q] = wread(q, 1); xc[q] = lds128(xa[0] + q * 2048); }
  xc[3] = lds128(xa[2]);
  qchunk(IC<0>{}, 0);
  STAMP(3);
  qchunk(IC<1>{}, 1);
#pragma unroll 1
  for (u32 c = 2; c < 6; ++c) qchunk(IC<2>{}, c);
  STAMP(4);
  qchunk(IC<3>{}, 6);
  qchunk(IC<4>{}, 7);
  STAMP(5);
  constexpr int IPOS[8] = {6, 0, 2, 4, 7, 1, 3, 5};
  const bool odd = wi & 1;
  bf16x8 qf[3][4];
#pragma unroll
  for (int q = 0; q < 3; ++q)
#pragma unroll
    for (int u = 0; u < 4; ++u) {
      const f32x4 a = o[q][IPOS[2 * u]] * QSCALE, bb = o[q][IPOS[2 * u + 1]] * QSCALE;
      const u32 a0 = cvtpk(a[0], a[1]), a1 = cvtpk(a[2], a[3]), b0 = cvtpk(bb[0], bb[1]), b1 = cvtpk(bb[2], bb[3]);
      auto s0 = __builtin_amdgcn_permlane16_swap(odd ? b0 : a0, odd ? a0 : b0, false, false);
      auto s1 = __builtin_amdgcn_permlane16_swap(odd ? b1 : a1, odd ? a1 : b1, false, false);
      qf[q][u] = asb(u32x4{s0[0], s1[0], s0[1], s1[1]});
    }
  u32 la16 = lane * 16;
  const u32 q12s = lds0 + Q12 + hh * 4096 + (wi >> 1) * 1024;
#pragma unroll
  for (int hf = 0; hf < 2; ++hf) {
    const f32x4 a = ox[hf] * QSCALE;
    *(LAS u32x2*)(size_t)(q12s + hf * 2048 + ((2 * (g >> 1) + (wi & 1)) * 16 + r16) * 16 + (g & 1) * 8) = u32x2{cvtpk(a[0], a[1]), cvtpk(a[2], a[3])};
  }
  auto q12frag = [&](int u) { return lds128(la16 + ((q12s ^ (u32)((u & 1) << 10)) + (u >> 1) * 2048)); };
  u32 ka[2], va[4];
  ka[0] = lds0 + hh * 8192 + r16 * 128 + ((((g >> 1) | ((wi >> 1) << 1)) ^ ((r16 >> 1) & 3)) * 32) + (g & 1) * 16;
  ka[1] = ka[0] ^ 64u;
  va[0] = lds0 + (hh ? V0B : V0A) - 16384 + (4 * g + q4) * 128 + ((((q4 >> 1) | ((g & 1) << 1)) ^ wi) * 32) + p4 * 8;
#pragma unroll
  for (int j = 1; j < 4; ++j) va[j] = va[0] ^ (u32)(j << 5);
  asm volatile("s_waitcnt lgkmcnt(0)\n\ts_barrier" ::: "memory");
  GAP();

  const u32 p12s = lds0 + P12 + hh * 4096;
  f32x4 negm[3], s0[3], s1[3];
  float m12;
  u32x4 p[3] = {}, px0;
  float l[4] = {0.f, 0.f, 0.f, 0.f};
  {
    const u32 k0[2] = {ka[0] + K0A, ka[1] + K0A};
    f32x4 t0, t1;
    bf16x8 kf[8], qx[4];
#pragma unroll
    for (int f = 0; f < 4; ++f) { kf[f] = kfrag(k0, f, 0); qx[f] = q12frag(f); }
    GAP();
    sfor<0, 8>([&](auto fc) {
      constexpr int f = decltype(fc)::v;
      if constexpr (f < 4) kf[f + 4] = kfrag(k0, f + 4, 0);
#pragma unroll
      for (int q = 0; q < 3; ++q) {
        if (f < 4) s0[q] = MFMA(kf[f], qf[q][f & 3], (f & 3) ? s0[q] : ZERO4);
        else s1[q] = MFMA(kf[f], qf[q][f & 3], (f & 3) ? s1[q] : ZERO4);
      }
      if (f < 4) t0 = MFMA(kf[f], qx[f & 3], (f & 3) ? t0 : ZERO4);
      else t1 = MFMA(kf[f], qx[f & 3], (f & 3) ? t1 : ZERO4);
      GAP();
    });
#pragma unroll
    for (int q = 0; q < 3; ++q) {
      const float mx = xmax_g(max8(s0[q], s1[q]));
      negm[q] = f32x4{-mx, -mx, -mx, -mx};
      s0[q] -= mx;
      s1[q] -= mx;
    }
    m12 = xmax_g(max8(t0, t1));
    float e[8];
#pragma unroll
    for (int i = 0; i < 8; ++i) e[i] = __builtin_amdgcn_exp2f((i < 4 ? t0[i & 3] : t1[i & 3]) - m12);
    l[3] = wi == 0 ? ((e[0] + e[1]) + (e[2] + e[3])) + ((e[4] + e[5]) + (e[6] + e[7])) : 0.f;
    px0 = u32x4{cvtpk(e[0], e[1]), cvtpk(e[2], e[3]), cvtpk(e[4], e[5]), cvtpk(e[6], e[7])};
    sfor<0, 50>([&](auto pc) { sm_ops<decltype(pc)::v>(s0, s1, p, l); });
  }
  STAMP(6);
  WAIT_BAR(8);
  STAMP(7);
  const u32 kt12 = (wi & 1) * 2048;
  bf16x8 kfc[3];
#pragma unroll
  for (int f = 0; f < 3; ++f) kfc[f] = kfrag(ka, f, 16384);
  if (wi < 2) {
    const u32 kx[2] = {ka[0] + kt12, ka[1] + kt12};
    f32x4 sy;
#pragma unroll
    for (int u = 0; u < 4; ++u) sy = MFMA(kfrag(kx, u, 16384), q12frag(u), u ? sy : ZERO4);
#pragma unroll
    for (int r = 0; r < 4; ++r) sy[r] = __builtin_amdgcn_exp2f(sy[r] - m12);
    l[3] += (sy[0] + sy[1]) + (sy[2] + sy[3]);
    *(LAS u32x2*)(size_t)(la16 + (p12s + 1024 + (wi & 1) * 8)) = u32x2{cvtpk(sy[0], sy[1]), cvtpk(sy[2], sy[3])};
  }
  const rsrc_t rk2 = mk_rsrc((br ? kB : kA) + (size_t)(KVIDX) * 512 * 128 + 4096, 512 * 128 * 2 - 8192);
  u32 so = 3 * 8192 + kvs;
  u32 lhk = kvl, lhv = kvl + VRING + 32768;
  u32 kxs = 32768 + kt12 + (wi >> 1) * 16384;
  u32 qs0 = q12s - p12s, qs1 = (q12s ^ 1024u) - p12s;
  u32 pa = la16 + p12s;
  const u32 pwo = (wi >> 1) * 1024 + (wi & 1) * 8;
  f32x4 sx;
  auto step = [&](auto oddc, auto firstc) {
    constexpr bool ODD = decltype(oddc)::v, FIRST = decltype(firstc)::v;
    constexpr u32 SO = ODD ? 16384 : 0, SN = ODD ? 0 : 16384;
    bf16x8 kf[8], kx[4], qx[4];
    bf16x4 vl[8], vh[8];
#pragma unroll
    for (int f = 0; f < 3; ++f) kf[f] = kfc[f];
    f32x4 n0[3], n1[3];
    u32 pxw[2];
    u32x4 px;
    u32 kxa[2], qa[2], pw;
    auto vread = [&](int i, int hi) {
      const u32 a = va[((i >> 1) + 1) & 3] + ((i & 1) * 4096 + SO + hi * 2048);
      if (hi) vh[i] = ldstr(a); else vl[i] = ldstr(a);
    };
    auto vfr = [&](int i) { return __builtin_shufflevector(vl[i], vh[i], 0, 1, 2, 3, 4, 5, 6, 7); };
    SETPRIO(1);
    GAP();
    sfor<0, 50>([&](auto jc) {
      constexpr int j = decltype(jc)::v;
      if constexpr (j < 24) {
        constexpr int f = j / 3, q = j % 3, u = f & 3;
        if constexpr (f < 4) n0[q] = MFMA(kf[f], qf[q][u], u == 0 ? negm[q] : n0[q]);
        else n1[q] = MFMA(kf[f], qf[q][u], u == 0 ? negm[q] : n1[q]);
      } else if constexpr (j < 48) {
        constexpr int i = j < 42 ? (j - 24) / 3 : 6 + (j & 1), q = j < 42 ? (j - 24) % 3 : (j - 42) / 2;
        o[q][i] = MFMA(vfr(i), asb(p[q]), FIRST ? ZERO4 : o[q][i]);
      } else {
        ox[j - 48] = MFMA(vfr(j - 42), asb(px), FIRST ? ZERO4 : ox[j - 48]);
      }
      GAP();
      if constexpr (j == 0) SET_M0(lhk, SN);
      if constexpr (j == 1) DMA_LD(voff, rk2, so, 0);
      if constexpr (j == 3) SET_M0(lhk, SN + 3968);
      if constexpr (j == 4) DMA_LD(voff, rk2, so, 128);
      if constexpr (j < 15 && j % 3 == 2) kf[3 + j / 3] = kfrag(ka, 3 + j / 3, SO);
      if constexpr (j == 12) SETPRIO(0);
      if constexpr (j >= 16 && j < 36 && (j - 16) % 3 < 2) vread((j - 16) / 3, (j - 16) % 3);
      if constexpr (j == 36 || j == 37) vread(7, j - 36);
      if constexpr (j >= 43 && (j - 43) % 3 == 0) kfc[(j - 43) / 3] = kfrag(ka, (j - 43) / 3, SN);
      if constexpr (j == 40) { if constexpr (FIRST) px = px0; else px = *(const LAS u32x4*)(size_t)(pa + (ODD ? 0 : 1024)); }
      if constexpr (!FIRST) {
        if constexpr (j == (ODD ? 44 : 24)) SET_M0(lhv, SN);
        if constexpr (j == (ODD ? 45 : 27)) DMA_LD(voff, rv, so, 0);
        if constexpr (j == (ODD ? 47 : 30)) SET_M0(lhv, SN + 3968);
        if constexpr (j == (ODD ? 48 : 33)) DMA_LD(voff, rv, so, 128);
      }
      if constexpr (j == 48) { so += 8192; PINS(so); }
      if constexpr (ODD) {
        if constexpr (j == 18 && !FIRST) *(LAS u32x2*)(size_t)pw = u32x2{pxw[0], pxw[1]};
        if constexpr (j == 24) kx[0] = kfrag(kxa, 0, 0);
        if constexpr (j == 27) qx[0] = lds128(qa[0]);
        if constexpr (j == 30) kx[1] = kfrag(kxa, 1, 0);
        if constexpr (j == 33) qx[1] = lds128(qa[1]);
        if constexpr (j == 38) kx[2] = kfrag(kxa, 2, 0);
        if constexpr (j == 39) qx[2] = lds128(qa[0] + 2048);
        if constexpr (j == 41) kx[3] = kfrag(kxa, 3, 0);
        if constexpr (j == 42) qx[3] = lds128(qa[1] + 2048);
      } else {
        if constexpr (j == 38) SETPRIO(2);
        if constexpr (j == 39) { lhk ^= 32768u; PINS(lhk); }
        if constexpr (j == 41) { lhv ^= 32768u; PINS(lhv); }
        if constexpr (j == 42) { kxs = kxs + ((lhk & 32768u) ? (u32)-65536 : 65536u); PINS(kxs); }
        if constexpr (j == 44) { qs0 ^= 0x3800u; PINS(qs0); }
        if constexpr (j == 45) { qs1 ^= 0x3800u; PINS(qs1); }
      }
      GAP();
      auto vflip = [&](int jj) { if (FIRST) va[jj] += VRING + hh * 8192 + 16384 - (hh ? V0B : V0A); else va[jj] ^= 32768u; PIN(va[jj]); };
      if constexpr (j < 12) sm_ops<50 + j>(s0, s1, p, l);
      else sm_ops<j>(n0, n1, p, l);
      if constexpr (ODD) {
        if constexpr (!FIRST) {
          if constexpr (j == 7) { pw = pa + pwo; PIN(pw); }
          if constexpr (j == 12 || j == 13) { sx[j - 10] = __builtin_amdgcn_exp2f(sx[j - 10]); PIN(sx); }
          if constexpr (j == 15) { pxw[0] = cvtpk(sx[0], sx[1]); PIN(pxw[0]); }
          if constexpr (j == 17) { pxw[1] = cvtpk(sx[2], sx[3]); PIN(pxw[1]); }
          if constexpr (j == 19 || j == 21) { l[3] += sx[2 + (j - 19) / 2]; PIN(l[3]); }
        }
        if constexpr (j == 23) { kxa[0] = ka[0] + kxs; PIN(kxa[0]); }
        if constexpr (j == 25) { qa[0] = pa + qs0; PIN(qa[0]); }
        if constexpr (j == 27) { kxa[1] = ka[1] + kxs; PIN(kxa[1]); }
        if constexpr (j == 29) { qa[1] = pa + qs1; PIN(qa[1]); }
        if constexpr (j == 31 || j == 33) { ka[(j - 31) / 2] ^= 32768u; PIN(ka[(j - 31) / 2]); }
        if constexpr (j == 39 || j == 41) vflip((j - 39) / 2);
      } else {
        if constexpr (j == 12 || j == 13) { sx[j - 12] = __builtin_amdgcn_exp2f(sx[j - 12] - m12); PIN(sx); }
        if constexpr (j == 15 || j == 17) { sx[2 + (j - 15) / 2] -= m12; PIN(sx); }
        if constexpr (j == 19 || j == 21) { l[3] += sx[(j - 19) / 2]; PIN(l[3]); }
        if constexpr (j == 41) { pa ^= 2048u; PIN(pa); }
      }
      GAP();
      if constexpr (ODD && (j == 32 || j == 38 || j == 44 || j == 47)) {
        constexpr int u = j == 32 ? 0 : (j == 38 ? 1 : (j == 44 ? 2 : 3));
        sx = MFMA(kx[u], qx[u], u == 0 ? ZERO4 : sx);
        GAP();
        if constexpr (j == 38) { SETPRIO(2); GAP(); vflip(2); }
        if constexpr (j == 44) vflip(3);
        GAP();
      }
      if constexpr (j == 23) {
        if constexpr (FIRST) asm volatile("s_waitcnt vmcnt(2)\n\ts_barrier" ::: "memory");
        else if constexpr (ODD) asm volatile("s_waitcnt vmcnt(4) lgkmcnt(4)\n\ts_barrier" ::: "memory");
        else asm volatile("s_waitcnt vmcnt(4)\n\ts_barrier" ::: "memory");
        SETPRIO(3);
        GAP();
      }
    });
#pragma unroll
    for (int q = 0; q < 3; ++q) s1[q] = n1[q];
  };
  step(IC<1>{}, IC<1>{});
  STAMP(8);
#pragma unroll 1
  for (u32 t = 2; t < 16; t += 2) {
    step(IC<0>{}, IC<0>{});
    step(IC<1>{}, IC<0>{});
  }
  STAMP(11);
  SETPRIO(0);
  la16 = (pa ^ 2048u) - p12s;
  P2 c2;
  {
    const u32 ln = la16 >> 4, m = (ln - 3) & 3, kq = xcd >> 1;
    const u32 sib = xcd + 8 * (b + 8 * ((hpl + 1 + m) & 3)), per = br + 2 * ((kq + 1 + m) & 3) + 8 * rr;
    c2.ldA = ln < 3 ? bid * 512 + 128 + ln * 4 : sib * 512;
    c2.stA = sib * 512 + 128 + ((2 - m) & 3) * 4;
    c2.ldB = ln < 3 ? bid * 512 + 256 + (ln * 8 + wid) * 4 : per * 512;
    c2.stB = per * 512 + 256 + (((2 - m) & 3) * 8 + wid) * 4;
    asm volatile("buffer_load_dword %0, %1, %2, 0 offen sc1" : "=v"(c2.nvA) : "v"(c2.ldA), "s"(rf));
    asm volatile("buffer_load_dword %0, %1, %2, 0 offen sc1" : "=v"(c2.nvB) : "v"(c2.ldB), "s"(rf));
  }
  {
    bf16x8 vf[8];
    const u32x4 px = *(const LAS u32x4*)(size_t)(la16 + (p12s + 3072));
#pragma unroll
    for (int i = 0; i < 4; ++i) vf[i] = vfrag(va, i, 0);
    GAP();
    sfor<50, 62>([&](auto pc) { sm_ops<decltype(pc)::v>(s0, s1, p, l); });
    GAP();
    bool bad = false;
    sfor<0, 8>([&](auto ic) {
      constexpr int i = decltype(ic)::v;
      if constexpr (i < 4) vf[i + 4] = vfrag(va, i + 4, 0);
#pragma unroll
      for (int q = 0; q < 3; ++q) o[q][i] = MFMA(vf[i], asb(p[q]), o[q][i]);
      if constexpr (i >= 6) ox[i - 6] = MFMA(vf[i], asb(px), ox[i - 6]);
      if constexpr (i < 4) { l[i] = xsum_g(l[i]); bad = bad || !(l[i] < 1.0e15f); }
      if constexpr (i == 4) {
        *(LAS float*)(size_t)(lds0 + FLG + hh * 256 + wi * 64 + ((la16 >> 2) & 60)) = l[3];
        *(LAS u32*)(size_t)(lds0 + FLG + 1024 + wid * 4) = __any(bad) ? 1u : 0u;
        if (wid == 0) *(LAS u32*)(size_t)(lds0 + FLG + 1056) = nonce;
      }
      GAP();
    });
  }
  asm volatile("s_waitcnt vmcnt(0) lgkmcnt(0)\n\ts_barrier" : "+v"(c2.nvA), "+v"(c2.nvB)::"memory");
  const u32 lsa = lds0 + FLG + hh * 256 + ((la16 >> 2) & 60);
  l[3] = (*(const LAS float*)(size_t)lsa + *(const LAS float*)(size_t)(lsa + 64)) + (*(const LAS float*)(size_t)(lsa + 128) + *(const LAS float*)(size_t)(lsa + 192));
  const u32x4 fl0 = *(const LAS u32x4*)(size_t)(lds0 + FLG + 1024), fl1 = *(const LAS u32x4*)(size_t)(lds0 + FLG + 1040);
  const u32 redo = __builtin_amdgcn_readfirstlane((fl0[0] | fl0[1] | fl0[2] | fl0[3]) | (fl1[0] | fl1[1] | fl1[2] | fl1[3]));
  c2.nonce = __builtin_amdgcn_readfirstlane(*(const LAS u32*)(size_t)(lds0 + FLG + 1056));
  const u32 kq = xcd >> 1;
  c2.ro = mk_rsrc(ows + ((size_t)(br * 8 + b) * 128 + kq * 32) * 13 * 512, 32 * 13312);
  c2.rw = mk_rsrc((br ? woB : woA) + (size_t)kq * 1024 * 512 + hpl * 128, 1024 * 1024 - hpl * 256);
  auto epilogue = [&]() {
    PIN(la16);
    {
      const u32 ln = la16 >> 4, voffW = (ln >> 3) * 1024 + ((((ln & 7) >> 1) ^ (ln >> 4)) * 32) + (ln & 1) * 16;
#pragma unroll
      for (int i = 0; i < 7; ++i) dma_wo(lds0 + i * SL2 + 13312 + hh * 4096 + wi * 1024, voffW, c2.rw, ((hpl * 4 + ((i / 2 + b) & 3)) * 64 + (i & 1) * 32 + wi * 8) * 1024 + hh * 128);
    }
    u32x4 w[3][4];
    u32x2 wx[2];
#pragma unroll
    for (int q = 0; q < 3; ++q) {
      const float inv = 1.0f / l[q];
#pragma unroll
      for (int u = 0; u < 4; ++u) {
        const f32x4 a = o[q][IPOS[2 * u]] * inv, bb = o[q][IPOS[2 * u + 1]] * inv;
        const u32 a0 = cvtpk(a[0], a[1]), a1 = cvtpk(a[2], a[3]), b0 = cvtpk(bb[0], bb[1]), b1 = cvtpk(bb[2], bb[3]);
        w[q][u] = u32x4{odd ? b0 : a0, odd ? b1 : a1, odd ? a0 : b0, odd ? a1 : b1};
      }
    }
    const float inv = 1.0f / l[3];
#pragma unroll
    for (int hf = 0; hf < 2; ++hf) {
      const f32x4 a = ox[hf] * inv;
      wx[hf] = u32x2{cvtpk(a[0], a[1]), cvtpk(a[2], a[3])};
    }
    asm volatile("s_waitcnt lgkmcnt(0)\n\ts_barrier" ::: "memory");
    auto slot_of = [&](u32 u) { const u32 e = hh * 4 + u; return lds0 + ((((e >> 1) - b) & 3) * 2 + (e & 1)) * SL2; };
#pragma unroll
    for (int u = 0; u < 4; ++u) {
      const u32 a = slot_of(u ^ (wi >> 1)) + wi * 3072;
#pragma unroll
      for (int q = 0; q < 3; ++q) *(LAS u32x4*)(size_t)(la16 + a + q * 1024) = w[q][u];
    }
#pragma unroll
    for (int hf = 0; hf < 2; ++hf) *(LAS u32x2*)(size_t)(la16 + slot_of(hf * 2 + (wi >> 1)) + 12288 + (wi & 1) * 8) = wx[hf];
  };
  STAMP(12);
  if (!redo) epilogue();
  STAMP(13);
  u32 redo2 = redo;
  PINS(redo2);
  if (redo2) {
    float m[4];
#pragma unroll
    for (int q = 0; q < 4; ++q) {
      l[q] = 0.f;
      m[q] = -3.0e38f;
    }
#pragma unroll
    for (int q = 0; q < 3; ++q)
#pragma unroll
      for (int dt = 0; dt < 8; ++dt) o[q][dt] = ZERO4;
    ox[0] = ox[1] = ZERO4;
#pragma unroll 1
    for (int t = 0; t < 16; ++t) {
      WAIT_BAR(0);
      kv_piece(rk, t, kvl, 0); kv_piece(rk, t, kvl, 1);
      kv_piece(rv, t, kvl + VRING + 32768, 0); kv_piece(rv, t, kvl + VRING + 32768, 1);
      WAIT_BAR(0);
      safe_block<8, 0>(ka, va, qf[0], o[0], m[0], l[0]);
      safe_block<8, 0>(ka, va, qf[1], o[1], m[1], l[1]);
      safe_block<8, 0>(ka, va, qf[2], o[2], m[2], l[2]);
      bf16x8 qx[4];
      PIN(la16);
#pragma unroll
      for (int q = 0; q < 4; ++q) qx[q] = q12frag(q);
      safe_block<2, 6>(ka, va, qx, ox, m[3], l[3]);
    }
#pragma unroll
    for (int q = 0; q < 4; ++q) l[q] = xsum_g(l[q]);
    WAIT_BAR(0);
    epilogue();
  }
  asm volatile("s_waitcnt vmcnt(5) lgkmcnt(0)\n\ts_barrier" ::: "memory");
  STAMP(14);
  c2.rp = mk_rsrc((char*)ows + WS_PART, 64 * 13 * 4 * 8192);
  c2.rf = rf;
  c2.wid = wid; c2.lane = la16 >> 4; c2.kq = kq; c2.j = hpl; c2.b = b;
  c2.pbase = ((br * 8 + b) * 4 + hpl) * (13 * 4 * 8192);
  c2.outp = out + (size_t)(b * 208) * 1024 + br * 512 + hpl * 128;
  if (wid < 4) p2_body<7>(c2 ST2PASS);
  else p2_body<6>(c2 ST2PASS);
}

extern "C" void kernel_launch(void* const* d_in, const int* in_sizes, int n_in, void* d_out, int out_size, void* d_ws, size_t ws_size, hipStream_t stream) {
  (void)in_sizes; (void)n_in; (void)out_size; (void)ws_size;
  (void)hipFuncSetAttribute((const void*)k1_qattn, hipFuncAttributeMaxDynamicSharedMemorySize, 163840);
  k1_qattn<<<256, 512, 163840, stream>>>((const u16*)d_in[0], (const u16*)d_in[1], (const u16*)d_in[2], (const u16*)d_in[3],
                                         (const u16*)d_in[5], (const u16*)d_in[6], (const u16*)d_in[7], (const u16*)d_in[8],
                                         (const u16*)d_in[4], (const u16*)d_in[9], (u16*)d_ws, (u16*)d_out);
}
```

```cpp
#include <hip/hip_runtime.h>
#include <hip/hip_bf16.h>

typedef unsigned short u16;
typedef unsigned int u32;
typedef __bf16 bf16x8 __attribute__((ext_vector_type(8)));
typedef __bf16 bf16x4 __attribute__((ext_vector_type(4)));
typedef __bf16 bf16x2 __attribute__((ext_vector_type(2)));
typedef float f32x4 __attribute__((ext_vector_type(4)));
typedef float f32x2 __attribute__((ext_vector_type(2)));
typedef u32 u32x4 __attribute__((ext_vector_type(4)));
#define LAS __attribute__((address_space(3)))
#define DI __device__ __forceinline__
typedef __amdgpu_buffer_rsrc_t rsrc_t;

#define MFMA(a, b, c) __builtin_amdgcn_mfma_f32_16x16x32_bf16((a), (b), (c), 0, 0, 0)
#define WAIT_BAR(N) asm volatile("s_waitcnt vmcnt(" #N ") lgkmcnt(0)\n\ts_barrier" ::: "memory")

DI rsrc_t mk_rsrc(const void* p, u32 bytes) { return __builtin_amdgcn_make_buffer_rsrc((void*)p, (short)0, (int)bytes, 0x00020000); }
DI void dma_nt(u32 lds, u32 voff, rsrc_t rs, u32 soff) {
  asm volatile("s_mov_b32 m0, %0\n\ts_nop 0\n\tbuffer_load_dwordx4 %1, %2, %3 offen nt lds" ::"s"(lds), "v"(voff), "s"(rs), "s"(soff));
}
DI void dma_df(u32 lds, u32 voff, rsrc_t rs, u32 soff) {
  asm volatile("s_mov_b32 m0, %0\n\ts_nop 0\n\tbuffer_load_dwordx4 %1, %2, %3 offen lds" ::"s"(lds), "v"(voff), "s"(rs), "s"(soff));
}
DI void dma_sc1(u32 lds, u32 voff, rsrc_t rs, u32 soff) {
  asm volatile("s_mov_b32 m0, %0\n\ts_nop 0\n\tbuffer_load_dwordx4 %1, %2, %3 offen sc1 lds" ::"s"(lds), "v"(voff), "s"(rs), "s"(soff));
}
#define ORD 0
#define AB_HOT 0
#define ACQ 1
#define NPUB 4
#define XPT 1
#define YPT 4
#define POL_XW 0
#define POL_O2 0
#define POL_WO 0
DI void dma_xw(u32 lds, u32 voff, rsrc_t rs, u32 soff) { if (POL_XW) dma_nt(lds, voff, rs, soff); else dma_df(lds, voff, rs, soff); }
DI void dma_o2(u32 lds, u32 voff, rsrc_t rs, u32 soff) { if (POL_O2) dma_nt(lds, voff, rs, soff); else dma_df(lds, voff, rs, soff); }
DI void dma_wo(u32 lds, u32 voff, rsrc_t rs, u32 soff) { if (POL_WO) dma_nt(lds, voff, rs, soff); else dma_df(lds, voff, rs, soff); }

DI bf16x8 lds128(u32 a) { return *(const LAS bf16x8*)(size_t)a; }
DI bf16x4 ldstr(u32 a) { return __builtin_amdgcn_ds_read_tr16_b64_v4bf16((LAS bf16x4*)(size_t)a); }
DI bf16x8 ldstr2(u32 a0, u32 a1) { bf16x4 lo = ldstr(a0), hi = ldstr(a1); return __builtin_shufflevector(lo, hi, 0, 1, 2, 3, 4, 5, 6, 7); }
DI u32 cvtpk(float lo, float hi) { f32x2 v = {lo, hi}; return __builtin_bit_cast(u32, __builtin_convertvector(v, bf16x2)); }
DI float xsum_g(float v) {
  auto a = __builtin_amdgcn_permlane16_swap(__float_as_uint(v), __float_as_uint(v), false, false);
  v = __uint_as_float(a[0]) + __uint_as_float(a[1]);
  auto b = __builtin_amdgcn_permlane32_swap(__float_as_uint(v), __float_as_uint(v), false, false);
  return __uint_as_float(b[0]) + __uint_as_float(b[1]);
}
DI float xmax_g(float v) {
  auto a = __builtin_amdgcn_permlane16_swap(__float_as_uint(v), __float_as_uint(v), false, false);
  v = fmaxf(__uint_as_float(a[0]), __uint_as_float(a[1]));
  auto b = __builtin_amdgcn_permlane32_swap(__float_as_uint(v), __float_as_uint(v), false, false);
  return fmaxf(__uint_as_float(b[0]), __uint_as_float(b[1]));
}

constexpr float QSCALE = 0.08838834764831845f * 1.4426950408889634f;
#define GAP() __builtin_amdgcn_sched_barrier(0)
#define PIN(x) asm volatile("" : "+v"(x))
#define ZERO4 (f32x4{0.f, 0.f, 0.f, 0.f})
template <int N> struct IC { static constexpr int v = N; };
template <int B, int E, class F> DI void sfor(F&& f) { if constexpr (B < E) { f(IC<B>{}); sfor<B + 1, E>(f); } }

#define STAMP(i)
#define ROT 3
#define ROT2 1
#define WOPF 0
#define PART_BF16 1
#define PRIO 0
#define KVIDX (b * 32 + h)
#define PD 4
constexpr u32 VRING = 65536, XBUF = 65536, XSZ = 26624, K0A = 131072, V0A = 147456, V0B = 122880, Q12 = 155648, P12 = 131072, FLG = 142336;
typedef u32 u32x2 __attribute__((ext_vector_type(2)));

DI float max8(const f32x4& a, const f32x4& b) { return fmaxf(fmaxf(fmaxf(a[0], a[1]), fmaxf(a[2], a[3])), fmaxf(fmaxf(b[0], b[1]), fmaxf(b[2], b[3]))); }
DI bf16x8 asb(const u32x4& w) { return __builtin_bit_cast(bf16x8, w); }
DI bf16x8 kfrag(const u32 (&ka)[2], int f, u32 off) { return lds128(ka[f & 1] + (((f >> 1) & 1) * 4096 + (f >> 2) * 2048 + off)); }
DI bf16x8 vfrag(const u32 (&va)[4], int i, u32 off) { const u32 a = va[((i >> 1) + 1) & 3] + ((i & 1) * 4096 + off); return ldstr2(a, a + 2048); }

template <int NT, int I0>
DI void safe_block(u32 (&ka)[2], u32 (&va)[4], const bf16x8 (&qf)[4], f32x4 (&o)[NT], float& m, float& l) {
  f32x4 s[2];
#pragma unroll
  for (int f = 0; f < 8; ++f) { s[f >> 2] = MFMA(kfrag(ka, f, 0), qf[f & 3], (f & 3) ? s[f >> 2] : ZERO4); GAP(); }
  const float mx = fmaxf(xmax_g(max8(s[0], s[1])), m);
  const float al = __builtin_amdgcn_exp2f(m - mx);
  m = mx;
  float e[8];
#pragma unroll
  for (int i = 0; i < 8; ++i) e[i] = __builtin_amdgcn_exp2f(s[i >> 2][i & 3] - mx);
  l = l * al + (((e[0] + e[1]) + (e[2] + e[3])) + ((e[4] + e[5]) + (e[6] + e[7])));
  u32x4 w = {cvtpk(e[0], e[1]), cvtpk(e[2], e[3]), cvtpk(e[4], e[5]), cvtpk(e[6], e[7])};
  GAP();
#pragma unroll
  for (int i = 0; i < NT; ++i) { o[i] *= al; PIN(o[i]); o[i] = MFMA(vfrag(va, I0 + i, 0), asb(w), o[i]); PIN(o[i]); GAP(); }
}

constexpr int EP(int k) { return 14 + 2 * k; }
constexpr int CW(int q, int w) { return w < 2 ? 43 + 2 * q : (q == 0 ? 49 + 2 * (w - 2) : (q == 1 ? 53 + 2 * (w - 2) : 59 + 2 * (w - 2))); }

template <int POS>
DI void sm_ops(f32x4 (&s0)[3], f32x4 (&s1)[3], u32x4 (&p)[3], float (&l)[4]) {
  sfor<0, 24>([&](auto kc) {
    constexpr int k = decltype(kc)::v, q = (k % 12) / 4, r = k & 3;
    if constexpr (EP(k) + 1 == POS) { l[q] += (k < 12) ? s0[q][r] : s1[q][r]; PIN(l[q]); }
    if constexpr (EP(k) == POS) {
      if constexpr (k < 12) { s0[q][r] = __builtin_amdgcn_exp2f(s0[q][r]); PIN(s0[q]); }
      else { s1[q][r] = __builtin_amdgcn_exp2f(s1[q][r]); PIN(s1[q]); }
    }
  });
  sfor<0, 12>([&](auto qc) {
    constexpr int q = decltype(qc)::v / 4, w = decltype(qc)::v & 3;
    if constexpr (CW(q, w) == POS) {
      if constexpr (w < 2) p[q][w] = cvtpk(s0[q][2 * w], s0[q][2 * w + 1]); else p[q][w] = cvtpk(s1[q][2 * w - 4], s1[q][2 * w - 3]);
      PIN(p[q]);
    }
  });
}
#define SET_M0(base, imm) asm volatile("s_add_u32 m0, %0, %1" ::"s"(base), "i"(imm) : "scc")
#define DMA_LD(voff, rs, soff, imm) asm volatile("buffer_load_dwordx4 %0, %1, %2 offen offset:%3 nt lds" ::"v"(voff), "s"(rs), "s"(soff), "i"(imm))
#define DMA_XW(voff, rs, soff, imm) asm volatile("buffer_load_dwordx4 %0, %1, %2 offen offset:%3 lds" ::"v"(voff), "s"(rs), "s"(soff), "i"(imm))
#define DMA_DF(voff, rs, soff, imm) asm volatile("buffer_load_dwordx4 %0, %1, %2 offen offset:%3 lds" ::"v"(voff), "s"(rs), "s"(soff), "i"(imm))
#define DMA_WO DMA_DF
#define DMA_O2 DMA_DF
#define PINS(x) asm volatile("" : "+s"(x))
#define SETPRIO(n)

constexpr u32 SL2 = 21504, XO7 = 7 * SL2;
constexpr size_t WS_PART = 32u << 20, WS_FLG = 30u << 20;
#define STAMP2(i)
#define ST2ARG
#define ST2PASS
DI f32x4 bf4(u32 lo, u32 hi) { return f32x4{__uint_as_float(lo << 16), __uint_as_float(lo & 0xffff0000u), __uint_as_float(hi << 16), __uint_as_float(hi & 0xffff0000u)}; }
DI u32 flag_ld(rsrc_t rf, u32 off) { return __builtin_amdgcn_raw_buffer_load_b32(rf, off, 0, 16); }
DI void flag_st(rsrc_t rf, u32 off, u32 v) { __builtin_amdgcn_raw_buffer_store_b32(v, rf, off, 0, 16); }
DI bool flag_pass(rsrc_t rf, u32 ld, u32 st, u32& nv, u32 nonce, u32 lane) {
  const u32 v = flag_ld(rf, ld);
  const bool chg = lane >= 3 && lane < 6 && v != nv;
  if (chg) { nv = v; flag_st(rf, st, v); }
  return __all(lane >= 3 || v == nonce);
}
DI void flag_wait(rsrc_t rf, u32 ld, u32 st, u32& nv, u32 nonce, u32 lane) {
  for (u32 spin = 0; spin < (1u << 21); ++spin) {
    if (flag_pass(rf, ld, st, nv, nonce, lane)) break;
    __builtin_amdgcn_s_sleep(1);
  }
}
struct P2 { rsrc_t ro, rw, rp, rf; u32 wid, lane, kq, j, b, pbase, nonce, ldA, stA, nvA, ldB, stB, nvB; u16* outp; };

template <int NB, int KQ>
DI void p2_tail(f32x4 (&acc)[NB][2], P2& c ST2ARG) {
  constexpr int B0 = (NB == 7) ? 0 : 7;
  constexpr u32 PS = PART_BF16 ? 4096 : 8192;
  const u32 lane = c.lane, la16 = lane * 16, g = lane >> 4;
#pragma unroll
  for (int q = 0; q < NB; ++q)
    if (((B0 + q) & 3) != KQ) {
      if (PART_BF16) {
        const u32x4 w = {cvtpk(acc[q][0][0], acc[q][0][1]), cvtpk(acc[q][0][2], acc[q][0][3]), cvtpk(acc[q][1][0], acc[q][1][1]), cvtpk(acc[q][1][2], acc[q][1][3])};
        __builtin_amdgcn_raw_buffer_store_b128(w, c.rp, la16, c.pbase + ((B0 + q) * 4 + KQ) * PS, 16);
      } else {
#pragma unroll
        for (int e = 0; e < 2; ++e)
          __builtin_amdgcn_raw_buffer_store_b128(__builtin_bit_cast(u32x4, acc[q][e]), c.rp, la16, c.pbase + ((B0 + q) * 4 + KQ) * PS + e * 1024, 16);
      }
    }
  STAMP2(3);
  asm volatile("s_waitcnt vmcnt(0)" ::: "memory");
  STAMP2(4);
  if (lane >= 3 && lane < 6) flag_st(c.rf, c.stB, c.nvB);
  if (c.wid == 0) flag_pass(c.rf, c.ldA, c.stA, c.nvA, c.nonce, lane);
  flag_wait(c.rf, c.ldB, c.stB, c.nvB, c.nonce, lane);
  asm volatile("" ::: "memory");
  STAMP2(5);
  u32x4 pv[2][3][2];
#pragma unroll
  for (int q = 0, n = 0; q < NB; ++q)
    if (((B0 + q) & 3) == KQ) {
#pragma unroll
      for (int s = 0; s < 3; ++s)
#pragma unroll
        for (int e = 0; e < (PART_BF16 ? 1 : 2); ++e)
          pv[n][s][e] = __builtin_amdgcn_raw_buffer_load_b128(c.rp, la16, c.pbase + ((B0 + q) * 4 + ((KQ + 1 + s) & 3)) * PS + e * 1024, 16);
      ++n;
    }
  flag_pass(c.rf, c.ldB, c.stB, c.nvB, c.nonce, lane);
  const u32 cg = ((g & 1) << 1) | (g >> 1);
#pragma unroll
  for (int q = 0, n = 0; q < NB; ++q)
    if (((B0 + q) & 3) == KQ) {
      f32x4 r[2];
#pragma unroll
      for (int e = 0; e < 2; ++e) {
        f32x4 t[3];
#pragma unroll
        for (int s = 0; s < 3; ++s) t[s] = PART_BF16 ? bf4(pv[n][s][0][2 * e], pv[n][s][0][2 * e + 1]) : __builtin_bit_cast(f32x4, pv[n][s][e]);
        r[e] = (acc[q][e] + t[0]) + (t[1] + t[2]);
      }
      auto s0 = __builtin_amdgcn_permlane16_swap(cvtpk(r[0][0], r[0][1]), cvtpk(r[1][0], r[1][1]), false, false);
      auto s1 = __builtin_amdgcn_permlane16_swap(cvtpk(r[0][2], r[0][3]), cvtpk(r[1][2], r[1][3]), false, false);
      *(u32x4*)(c.outp + (size_t)((B0 + q) * 16) * 1024 + cg * 8) = u32x4{s0[0], s1[0], s0[1], s1[1]};
      ++n;
    }
  STAMP2(6);
  STAMP2(7);
}

template <int NB>
DI void p2_body(P2& c ST2ARG) {
  const u32 lane = c.lane, wid = c.wid;
  const u32 g = lane >> 4, r16 = lane & 15, q4 = r16 >> 2, p4 = lane & 3;
  const u32 j3 = lane >> 3, j7 = lane & 7, j4 = lane >> 4;
  const u32 np = wid & 3;
  const u32 voffO = lane * 16, voffO8 = voffO + 8192, voffO16 = voffO + 16384;
  const u32 voffW = j3 * 1024 + (((j7 >> 1) ^ j4) * 32) + (lane & 1) * 16;
  u32 s0 = SL2, s1 = 2 * SL2, jd = 4, pjn;
  auto pair_of = [&](u32 q) { const u32 m = q >> 2, bl = ORD ? (m == 0 ? c.j : (m - 1 < c.j ? m - 1 : m)) : ((c.j + m) & 3); return q < 16 ? (bl << 2) | ((q + c.b) & 3) : 64u; };
  u32 sw = wid * 8192 + pair_of(4) * 65536, so = wid * 1024 + pair_of(4) * 26624;
  const u32 cw = 13312 + np * 1024, c3 = wid < 5 ? wid * 1024 + 8192 : wid * 1024 - 5120;
  auto dop = [&](int k) {
    if (k == 0) { const u32 m = (NB == 7 ? s0 : s1) + cw; SET_M0(m, 0); }
    if (k == 1) DMA_WO(voffW, c.rw, sw, 0);
    if (k == 2) { const u32 m = (NB == 7 ? s0 : s1) + cw; SET_M0(m, 3968); }
    if (k == 3) DMA_WO(voffW, c.rw, sw, 128);
    if (k == 4) { const u32 m = s0 + wid * 1024; SET_M0(m, 0); }
    if (k == 5 && !(AB_HOT & 8)) DMA_O2(voffO, c.ro, so, 0);
    if (k == 6) { const u32 m = (wid < 5 ? s0 : s1) + c3; SET_M0(m, 0); }
    if (k == 7 && !(AB_HOT & 8)) DMA_O2(voffO8, c.ro, so, 0);
    if (k == 8) { const u32 m = s1 + wid * 1024; SET_M0(m, 3072); }
    if (k == 9 && !(AB_HOT & 24)) DMA_O2(voffO16, c.ro, so, 0);
    if (k == 10) { if (NB == 7 && wid < 2) { const u32 m = s1 + wid * 1024 + 11264, so5 = so + 24576; dma_df(m, voffO, c.ro, so5); } }
    if (k == 11) { ++jd; pjn = pair_of(jd); PINS(jd); PINS(pjn); }
    if (k == 12) { sw = wid * 8192 + ((AB_HOT & 1) && pjn < 64 ? (pjn & 1) : pjn) * 65536; PINS(sw); }
    if (k == 13) { so = wid * 1024 + ((AB_HOT & 4) ? 64 : ((AB_HOT & 2) && pjn < 64 ? (pjn & 1) : pjn)) * 26624; PINS(so); }
    if (k == 14) { s0 = (s0 >= 5 * SL2) ? s0 - 5 * SL2 : s0 + 2 * SL2; PINS(s0); }
    if (k == 15) { s1 = (s1 >= 5 * SL2) ? s1 - 5 * SL2 : s1 + 2 * SL2; PINS(s1); }
  };
  u32 oa = (NB == 7 ? 0 : 7168) + lane * 16;
  u32 wa = 13312 + (np >> 1) * 4096 + (4 * g + q4) * 128 + ((((q4 >> 1) | ((g & 1) << 1)) ^ ((np & 1) * 2)) * 32) + p4 * 8;
  u32 wb = wa ^ 32u;
  f32x4 acc[NB][2];
#pragma unroll
  for (int q = 0; q < NB; ++q) acc[q][0] = acc[q][1] = ZERO4;
  bf16x4 wl[2], wh[2];
  bf16x8 oc[NB];
  wl[0] = ldstr(wa); wh[0] = ldstr(wa + 2048); wl[1] = ldstr(wb); wh[1] = ldstr(wb + 2048);
#pragma unroll
  for (int q = 0; q < NB; ++q) oc[q] = lds128(oa + q * 1024);
  u32 ks = 0, rs = 0;
  auto kstep = [&](auto modec) {
    constexpr int MODE = decltype(modec)::v;
    bf16x8 on[NB];
    bf16x4 nl[2], nh[2];
    const bf16x8 w0 = __builtin_shufflevector(wl[0], wh[0], 0, 1, 2, 3, 4, 5, 6, 7), w1 = __builtin_shufflevector(wl[1], wh[1], 0, 1, 2, 3, 4, 5, 6, 7);
    const u32 d = (rs == 6) ? (u32)(-6 * (int)SL2) : SL2;
    const u32 d2 = (MODE == 0 || MODE == 4) ? (ks == 6 ? SL2 : d) : ((MODE == 1 && ks == 7) ? (u32)(-6 * (int)SL2) : d);
    rs = (rs == 6) ? 0 : rs + 1;
    ++ks;
    GAP();
    sfor<0, 2 * NB>([&](auto nc) {
      constexpr int n = decltype(nc)::v, q = n >> 1;
      constexpr int gs = MODE == 1 ? n - 1 : (MODE == 2 ? 2 * NB - 1 + n : -1);
      if constexpr (n & 1) acc[q][1] = MFMA(w1, oc[q], acc[q][1]); else acc[q][0] = MFMA(w0, oc[q], acc[q][0]);
      GAP();
      if constexpr (n == 0) {
        wa += d; wb += d; oa += d2; PIN(wa); PIN(wb); PIN(oa);
        if constexpr (MODE == 1 || MODE == 3) asm volatile("s_waitcnt vmcnt(%0)\n\ts_barrier" ::"i"((AB_HOT & 8) ? 2 : ((AB_HOT & 16) ? 4 : 5)) : "memory");
      }
      if constexpr (n == 1) nl[0] = ldstr(wa);
      if constexpr (n == 2) nh[0] = ldstr(wa + 2048);
      if constexpr (n == 3) on[0] = lds128(oa);
      if constexpr (n == 4) nl[1] = ldstr(wb);
      if constexpr (n == 5) nh[1] = ldstr(wb + 2048);
      if constexpr (n >= 6 && n - 5 < NB) on[n - 5] = lds128(oa + (n - 5) * 1024);
      if constexpr (MODE == 4) { if constexpr (n < 11) dop(n); if constexpr (n == 2 * NB - 1) { dop(11); dop(12); dop(13); dop(14); dop(15); } }
      if constexpr (gs >= 0 && gs < 24 && (gs & 3) < 2 && (gs >> 2) * 2 + (gs & 3) < 11) dop((gs >> 2) * 2 + (gs & 3));
      if constexpr (gs == 6) dop(11);
      if constexpr (gs == 4 * NB - 6) dop(12);
      if constexpr (gs == 4 * NB - 5) dop(13);
      if constexpr (gs == 4 * NB - 4) dop(14);
      if constexpr (gs == 4 * NB - 3) dop(15);
      GAP();
    });
#pragma unroll
    for (int q = 0; q < NB; ++q) oc[q] = on[q];
    wl[0] = nl[0]; wh[0] = nh[0]; wl[1] = nl[1]; wh[1] = nh[1];
  };
  STAMP2(1);
  __builtin_amdgcn_raw_buffer_store_b128(*(const LAS u32x4*)(size_t)(wid * SL2 + 12288 + voffO), c.ro, voffO, (pair_of(wid >> 1) * 2 + (wid & 1)) * 13312 + 12288, 16);
#pragma unroll 1
  for (u32 i = 0; i < 7; ++i) {
    if (i == YPT + 1) kstep(IC<4>{}); else kstep(IC<0>{});
    if (i == XPT) {
      asm volatile("s_waitcnt vmcnt(0)\n\ts_barrier" ::: "memory");
      if (wid == 0 && lane >= 3 && lane < 6) flag_st(c.rf, c.stA, c.nvA);
      dma_nt(6 * SL2 + 13312 + (wid >> 2) * 4096 + np * 1024, voffW, c.rw, (pair_of(3) * 64 + np * 8) * 1024 + (wid >> 2) * 128);
      dma_nt(13312 + (wid >> 2) * 4096 + np * 1024, voffW, c.rw, (pair_of(3) * 64 + 32 + np * 8) * 1024 + (wid >> 2) * 128);
    }
    if (i == YPT) {
      if (wid == 0) {
        flag_wait(c.rf, c.ldA, c.stA, c.nvA, c.nonce, lane);
        if (ACQ) __builtin_amdgcn_fence(__ATOMIC_ACQUIRE, "agent");
      }
      asm volatile("s_waitcnt vmcnt(0) lgkmcnt(0)\n\ts_barrier" ::: "memory");
      STAMP2(0);
#pragma unroll
      for (int k = 0; k < 16; ++k) { dop(k); if (!(k & 1) && k < 10) asm volatile("s_nop 0"); }
    }
  }
#pragma unroll 1
  for (u32 jp = 3; jp < 15; ++jp) { kstep(IC<1>{}); kstep(IC<2>{}); }
  kstep(IC<3>{});
  STAMP2(2);
  c.outp += (size_t)r16 * 1024 + np * 32;
  c.pbase += np * (PART_BF16 ? 1024 : 2048);
  if (c.kq == 0) p2_tail<NB, 0>(acc, c ST2PASS);
  else if (c.kq == 1) p2_tail<NB, 1>(acc, c ST2PASS);
  else if (c.kq == 2) p2_tail<NB, 2>(acc, c ST2PASS);
  else p2_tail<NB, 3>(acc, c ST2PASS);
}

__global__ __launch_bounds__(512, 2) void k1_qattn(const u16* __restrict__ xA, const u16* __restrict__ kA, const u16* __restrict__ vA, const u16* __restrict__ wqA,
                                                   const u16* __restrict__ xB, const u16* __restrict__ kB, const u16* __restrict__ vB, const u16* __restrict__ wqB,
                                                   const u16* __restrict__ woA, const u16* __restrict__ woB, u16* __restrict__ ows, u16* __restrict__ out) {
  extern __shared__ __attribute__((aligned(16))) char smem[];
  const u32 bid = blockIdx.x, xcd = bid & 7, rr = bid >> 3;
  const u32 br = xcd & 1, b = rr & 7, hpl = rr >> 3, hp = (xcd >> 1) * 4 + hpl;
  const u32 lane = threadIdx.x & 63;
  const u32 wid = __builtin_amdgcn_readfirstlane(threadIdx.x >> 6);
  const u32 hh = wid >> 2, wi = wid & 3, h = hp * 2 + hh;
  constexpr u32 RM = ROT == 3 ? 3 : (ROT == 4 ? 1 : 7);
  const u32 c0 = ROT == 1 ? (b + 2 * hpl) & 7 : (ROT == 2 ? ((b ^ hpl) & 1) * 4 : (ROT == 3 ? (b + hpl) & 3 : (ROT == 4 ? (b + hpl) & 1 : 0)));
  auto chunk_of = [&](u32 c) { return (c & ~RM) | ((c + c0) & RM); };
  const u32 lds0 = (u32)(size_t)(LAS char*)smem;
  const rsrc_t rx = mk_rsrc((br ? xB : xA) + (size_t)b * 208 * 512, 208 * 512 * 2);
  const rsrc_t rw = mk_rsrc(br ? wqB : wqA, 512 * 4096 * 2);
  const rsrc_t rk = mk_rsrc((br ? kB : kA) + (size_t)(KVIDX) * 512 * 128, 512 * 128 * 2);
  const rsrc_t rv = mk_rsrc((br ? vB : vA) + (size_t)(KVIDX) * 512 * 128, 512 * 128 * 2);
  STAMP(0);
  const rsrc_t rf = mk_rsrc((char*)ows + WS_FLG, 256 * 512);
  u32 inbox;
  asm volatile("s_nop 4\n\tbuffer_load_dword %0, %1, %2, 0 offen sc1" : "=v"(inbox) : "v"(bid * 512 + (lane < 28 ? (lane < 4 ? (lane ? 124 + lane * 4 : 0) : 240 + lane * 4) : 0)), "s"(rf));

  const u32 g = lane >> 4, r16 = lane & 15, q4 = r16 >> 2, p4 = lane & 3;
  const u32 j3 = lane >> 3, j7 = lane & 7, j4 = lane >> 4;
  const u32 voffX = j3 * 1024 + ((j7 ^ ((wid & 1) * 4 + j4)) * 16);
  const u32 voffW = j3 * 8192 + ((j7 ^ ((j4 & 1) | ((wid & 1) << 2))) * 16);
  const u32 voff = j3 * 256 + (((j7 >> 1) ^ j4) * 32) + (lane & 1) * 16;
  const u32 kvl = lds0 + hh * 8192 + wi * 1024;
  const u32 kvs = wi * 2048;
  auto kv_piece = [&](rsrc_t rs, u32 step, u32 lb, int hf) { dma_nt(lb + hf * 4096, voff, rs, step * 8192 + kvs + hf * 128); };
  const u32 swb = wi * 65536 + h * 256;
  u32 jw = 0, jx = 0, sw = c0 * 524288 + swb, lw = kvl + 16384;
  u32 sx_ = wid * 8192 + c0 * 128, lx = lds0 + XBUF + wid * 1024, lx3 = wid < 2 ? lx + 24576 : lds0 + Q12 + wid * 1024;
  const u32 dx3 = wid < 2 ? XSZ : 0;
  u32 voffXi[4];
#pragma unroll
  for (int i = 0; i < 4; ++i) voffXi[i] = voffX + i * 65536;
  auto w_m0 = [&](int hf) { SET_M0(lw, hf * 3968); };
  auto w_ld = [&](int hf) { DMA_XW(voffW, rw, sw, hf * 128); };
  auto w_adv = [&](int ph) { if (ph == 0) { ++jw; sw = chunk_of(jw >> 1) * 524288 + (jw & 1) * 262144 + swb; PINS(sw); PINS(jw); } else { lw = (lw + 16384) & 0xffffu; PINS(lw); } };
  auto x_m0 = [&](int i) { if (i < 3) SET_M0(lx, i * 8192); else SET_M0(lx3, 0); };
  auto x_ld = [&](int i) { DMA_XW(voffXi[i], rx, sx_, 0); };
  auto x_adv = [&](u32 c) {
    ++jx; sx_ = wid * 8192 + chunk_of(jx) * 128; PINS(jx);
    lx += (c & 1) ? (u32)-XSZ : XSZ; lx3 += (c & 1) ? (u32)-dx3 : dx3;
    PINS(sx_); PINS(lx); PINS(lx3);
  };
  auto nop1 = [&]() { asm volatile("s_nop 0"); };
  const u32 v0l = lds0 + (hh ? V0B : V0A) + wi * 1024;
  w_m0(0); nop1(); w_ld(0); w_m0(1); nop1(); w_ld(1); w_adv(0); w_adv(1);
#pragma unroll
  for (int i = 0; i < 4; ++i) { x_m0(i); nop1(); x_ld(i); }
  x_adv(0);
  w_m0(0); nop1(); w_ld(0); w_m0(1); nop1(); w_ld(1); w_adv(0); w_adv(1);

  u32 xa[3], wa[4];
  xa[0] = lds0 + XBUF + wi * 6144 + r16 * 128 + ((g ^ (r16 >> 1)) * 16);
  xa[1] = xa[0] ^ 64u;
  xa[2] = xa[0] + (4 - wi) * 6144;
  wa[0] = lds0 + 16384 + hh * 8192 + (8 * g + q4) * 128 + ((((2 * (p4 >> 1)) | (wi & 1) | ((wi >> 1) << 2)) ^ ((q4 >> 1) | ((g & 1) << 2))) * 16) + (p4 & 1) * 8;
#pragma unroll
  for (int j = 1; j < 4; ++j) wa[j] = wa[0] ^ (u32)(((j & 1) << 4) | ((j >> 1) << 6));
  f32x4 o[3][8], ox[2];
  auto wread = [&](int n, int hi) { return ldstr(wa[(((n & 7) >> 1) + 1) & 3] + ((n & 1) * 4096 + hi * 512)); };
  bf16x8 xc[4];
  bf16x4 wcl[3], wch[3];
  auto qchunk = [&](auto kindc, u32 c) {
    constexpr int KIND = decltype(kindc)::v;
    bf16x8 xf[2][4];
    bf16x4 wl[19], wh[19];
    u32 x13;
#pragma unroll
    for (int q = 0; q < 4; ++q) xf[0][q] = xc[q];
#pragma unroll
    for (int q = 0; q < 3; ++q) { wl[q] = wcl[q]; wh[q] = wch[q]; }
    const u32 dw = (c & 1) ? (u32)-49152 : 16384u, dx = (c & 1) ? (u32)-XSZ : XSZ;
    GAP();
    sfor<0, 16>([&](auto nc) {
      constexpr int n = decltype(nc)::v, i = n & 7;
      constexpr bool RD = (n + 3 < 16 || KIND != 4);
      const bf16x8 wf = __builtin_shufflevector(wl[n], wh[n], 0, 1, 2, 3, 4, 5, 6, 7);
      auto dma_op = [&](auto kc) {
        constexpr int z = decltype(kc)::v;
        auto two = [&](auto m0f, auto ldf) { if constexpr (z == 0) m0f(); else ldf(); };
        if constexpr (KIND == 0 && n < 4) {
          constexpr int y = 2 * n + z;
          if constexpr (y == 0) w_m0(0); if constexpr (y == 1) { w_ld(0); w_m0(1); } if constexpr (y == 2) { w_ld(1); w_adv(0); w_adv(1); x_m0(0); }
          if constexpr (y >= 3 && y < 6) { x_ld(y - 3); x_m0(y - 2); } if constexpr (y == 6) { x_ld(3); x_adv(1); }
        }
        if constexpr (KIND >= 1 && KIND <= 3) {
          if constexpr (n == 0 || n == 2) two([&] { x_m0(2 + n / 2); }, [&] { x_ld(2 + n / 2); });
          if constexpr (n == 3 && z == 0) x_adv(c + 1);
        }
        if constexpr (KIND == 1 && n == 3 && z == 1) { kv_piece(rk, 0, kvl + K0A, 0); kv_piece(rk, 0, kvl + K0A, 1); kv_piece(rv, 0, v0l, 0); kv_piece(rv, 0, v0l, 1); }
        if constexpr ((KIND <= 3 && (n == 4 || n == 6)) || (KIND <= 2 && (n == 12 || n == 14))) two([&] { w_m0((n >> 1) & 1); }, [&] { w_ld((n >> 1) & 1); });
        if constexpr ((KIND <= 3 && n == 7) || (KIND <= 2 && n == 15)) w_adv(z);
        if constexpr (KIND <= 2 && (n == 8 || n == 10)) two([&] { x_m0((n - 8) / 2); }, [&] { x_ld((n - 8) / 2); });
        if constexpr (z == 1) {
          if constexpr (KIND == 3 && (n == 8 || n == 10)) kv_piece(rv, 1, kvl + VRING, (n >> 1) & 1);
          if constexpr (KIND == 3 && (n == 12 || n == 14)) kv_piece(rk, 1, kvl + 16384, (n >> 1) & 1);
          if constexpr (KIND == 4 && (n == 4 || n == 5)) kv_piece(rk, 2, kvl + 32768, n & 1);
          if constexpr (KIND == 4 && n >= 6 && n < 10) kv_piece(rv, n / 2 - 1, kvl + VRING + (n / 2 - 2) * 16384, n & 1);
          if constexpr (KIND == 4 && (n == 12 || n == 14)) kv_piece(rk, 3, kvl + 49152, (n >> 1) & 1);
        }
      };
      o[0][i] = MFMA(wf, xf[n >> 3][0], (KIND == 0 && n < 8) ? ZERO4 : o[0][i]);
      GAP();
      if constexpr (RD) { wl[n + 3] = wread(n + 3, 0); wh[n + 3] = wread(n + 3, 1); }
      GAP();
      o[1][i] = MFMA(wf, xf[n >> 3][1], (KIND == 0 && n < 8) ? ZERO4 : o[1][i]);
      GAP();
      if constexpr (n < 3) xf[1][n] = lds128(xa[1] + n * 2048);
      if constexpr (KIND != 4 && n >= 12 && n < 15) xc[n - 12] = lds128(xa[0] + (n - 12) * 2048);
      dma_op(IC<0>{});
      GAP();
      if constexpr (n == 0) { x13 = xa[2] ^ 64u; PIN(x13); }
      if constexpr (n == 4 || (n == 12 && KIND != 4)) { wa[0] += (n == 4 ? dw : 16384u); wa[1] += (n == 4 ? dw : 16384u); PIN(wa[0]); PIN(wa[1]); }
      if constexpr (n == 6 && KIND != 4) { xa[0] += dx; xa[1] += dx; PIN(xa[0]); PIN(xa[1]); }
      GAP();
      o[2][i] = MFMA(wf, xf[n >> 3][2], (KIND == 0 && n < 8) ? ZERO4 : o[2][i]);
      GAP();
      if constexpr (n == 1) xf[1][3] = lds128(x13);
      if constexpr (KIND != 4 && n == 15) xc[3] = lds128(xa[2]);
      dma_op(IC<1>{});
      GAP();
      if constexpr (n == 4 || (n == 12 && KIND != 4)) { wa[2] += (n == 4 ? dw : 16384u); wa[3] += (n == 4 ? dw : 16384u); PIN(wa[2]); PIN(wa[3]); }
      if constexpr (n == 6 && KIND != 4) { xa[2] += dx; PIN(xa[2]); }
      GAP();
      if constexpr (i >= 6) { ox[i - 6] = MFMA(wf, xf[n >> 3][3], (KIND == 0 && n < 8) ? ZERO4 : ox[i - 6]); GAP(); }
      if constexpr (n == 3 || n == 11) {
        if constexpr (KIND == 4 && n == 11) asm volatile("s_barrier" ::: "memory");
        else asm volatile("s_waitcnt vmcnt(%0) lgkmcnt(2)\n\ts_barrier" ::"i"((n == 3 && KIND != 4 ? 6 : 4) + (KIND == 1 ? 4 : 0)) : "memory");
        GAP();
      }
    });
#pragma unroll
    for (int q = 0; q < 3; ++q) { wcl[q] = wl[16 + q]; wch[q] = wh[16 + q]; }
  };
  STAMP(1);
  asm volatile("s_waitcnt vmcnt(2) lgkmcnt(0)\n\ts_barrier" : "+v"(inbox)::"memory");
  STAMP(2);
  u32 nonce = __builtin_amdgcn_readfirstlane(inbox) + 1;
  if (wid == 0) {
    while (__any(inbox == nonce)) ++nonce;
    if (lane == 0) flag_st(rf, bid * 512, nonce);
  }
#pragma unroll
  for (int q = 0; q < 3; ++q) { wcl[q] = wread(q, 0); wch[q] = wread(q, 1); xc[q] = lds128(xa[0] + q * 2048); }
  xc[3] = lds128(xa[2]);
  qchunk(IC<0>{}, 0);
  STAMP(3);
  qchunk(IC<1>{}, 1);
#pragma unroll 1
  for (u32 c = 2; c < 6; ++c) qchunk(IC<2>{}, c);
  STAMP(4);
  qchunk(IC<3>{}, 6);
  qchunk(IC<4>{}, 7);
  STAMP(5);
  constexpr int IPOS[8] = {6, 0, 2, 4, 7, 1, 3, 5};
  const bool odd = wi & 1;
  bf16x8 qf[3][4];
#pragma unroll
  for (int q = 0; q < 3; ++q)
#pragma unroll
    for (int u = 0; u < 4; ++u) {
      const f32x4 a = o[q][IPOS[2 * u]] * QSCALE, bb = o[q][IPOS[2 * u + 1]] * QSCALE;
      const u32 a0 = cvtpk(a[0], a[1]), a1 = cvtpk(a[2], a[3]), b0 = cvtpk(bb[0], bb[1]), b1 = cvtpk(bb[2], bb[3]);
      auto s0 = __builtin_amdgcn_permlane16_swap(odd ? b0 : a0, odd ? a0 : b0, false, false);
      auto s1 = __builtin_amdgcn_permlane16_swap(odd ? b1 : a1, odd ? a1 : b1, false, false);
      qf[q][u] = asb(u32x4{s0[0], s1[0], s0[1], s1[1]});
    }
  u32 la16 = lane * 16;
  const u32 q12s = lds0 + Q12 + hh * 4096 + (wi >> 1) * 1024;
#pragma unroll
  for (int hf = 0; hf < 2; ++hf) {
    const f32x4 a = ox[hf] * QSCALE;
    *(LAS u32x2*)(size_t)(q12s + hf * 2048 + ((2 * (g >> 1) + (wi & 1)) * 16 + r16) * 16 + (g & 1) * 8) = u32x2{cvtpk(a[0], a[1]), cvtpk(a[2], a[3])};
  }
  auto q12frag = [&](int u) { return lds128(la16 + ((q12s ^ (u32)((u & 1) << 10)) + (u >> 1) * 2048)); };
  u32 ka[2], va[4];
  ka[0] = lds0 + hh * 8192 + r16 * 128 + ((((g >> 1) | ((wi >> 1) << 1)) ^ ((r16 >> 1) & 3)) * 32) + (g & 1) * 16;
  ka[1] = ka[0] ^ 64u;
  va[0] = lds0 + (hh ? V0B : V0A) - 16384 + (4 * g + q4) * 128 + ((((q4 >> 1) | ((g & 1) << 1)) ^ wi) * 32) + p4 * 8;
#pragma unroll
  for (int j = 1; j < 4; ++j) va[j] = va[0] ^ (u32)(j << 5);
  asm volatile("s_waitcnt lgkmcnt(0)\n\ts_barrier" ::: "memory");
  GAP();

  const u32 p12s = lds0 + P12 + hh * 4096;
  f32x4 negm[3], s0[3], s1[3];
  float m12;
  u32x4 p[3] = {}, px0;
  float l[4] = {0.f, 0.f, 0.f, 0.f};
  {
    const u32 k0[2] = {ka[0] + K0A, ka[1] + K0A};
    f32x4 t0, t1;
    bf16x8 kf[8], qx[4];
#pragma unroll
    for (int f = 0; f < 4; ++f) { kf[f] = kfrag(k0, f, 0); qx[f] = q12frag(f); }
    GAP();
    sfor<0, 8>([&](auto fc) {
      constexpr int f = decltype(fc)::v;
      if constexpr (f < 4) kf[f + 4] = kfrag(k0, f + 4, 0);
#pragma unroll
      for (int q = 0; q < 3; ++q) {
        if (f < 4) s0[q] = MFMA(kf[f], qf[q][f & 3], (f & 3) ? s0[q] : ZERO4);
        else s1[q] = MFMA(kf[f], qf[q][f & 3], (f & 3) ? s1[q] : ZERO4);
      }
      if (f < 4) t0 = MFMA(kf[f], qx[f & 3], (f & 3) ? t0 : ZERO4);
      else t1 = MFMA(kf[f], qx[f & 3], (f & 3) ? t1 : ZERO4);
      GAP();
    });
#pragma unroll
    for (int q = 0; q < 3; ++q) {
      const float mx = xmax_g(max8(s0[q], s1[q]));
      negm[q] = f32x4{-mx, -mx, -mx, -mx};
      s0[q] -= mx;
      s1[q] -= mx;
    }
    m12 = xmax_g(max8(t0, t1));
    float e[8];
#pragma unroll
    for (int i = 0; i < 8; ++i) e[i] = __builtin_amdgcn_exp2f((i < 4 ? t0[i & 3] : t1[i & 3]) - m12);
    l[3] = wi == 0 ? ((e[0] + e[1]) + (e[2] + e[3])) + ((e[4] + e[5]) + (e[6] + e[7])) : 0.f;
    px0 = u32x4{cvtpk(e[0], e[1]), cvtpk(e[2], e[3]), cvtpk(e[4], e[5]), cvtpk(e[6], e[7])};
    sfor<0, 50>([&](auto pc) { sm_ops<decltype(pc)::v>(s0, s1, p, l); });
  }
  STAMP(6);
  WAIT_BAR(8);
  STAMP(7);
  const u32 kt12 = (wi & 1) * 2048;
  bf16x8 kfc[3];
#pragma unroll
  for (int f = 0; f < 3; ++f) kfc[f] = kfrag(ka, f, 16384);
  if (wi < 2) {
    const u32 kx[2] = {ka[0] + kt12, ka[1] + kt12};
    f32x4 sy;
#pragma unroll
    for (int u = 0; u < 4; ++u) sy = MFMA(kfrag(kx, u, 16384), q12frag(u), u ? sy : ZERO4);
#pragma unroll
    for (int r = 0; r < 4; ++r) sy[r] = __builtin_amdgcn_exp2f(sy[r] - m12);
    l[3] += (sy[0] + sy[1]) + (sy[2] + sy[3]);
    *(LAS u32x2*)(size_t)(la16 + (p12s + 1024 + (wi & 1) * 8)) = u32x2{cvtpk(sy[0], sy[1]), cvtpk(sy[2], sy[3])};
  }
  const rsrc_t rk2 = mk_rsrc((br ? kB : kA) + (size_t)(KVIDX) * 512 * 128 + 4096, 512 * 128 * 2 - 8192);
  u32 so = 3 * 8192 + kvs;
  u32 lhk = kvl, lhv = kvl + VRING + 32768;
  u32 kxs = 32768 + kt12 + (wi >> 1) * 16384;
  u32 qs0 = q12s - p12s, qs1 = (q12s ^ 1024u) - p12s;
  u32 pa = la16 + p12s;
  const u32 pwo = (wi >> 1) * 1024 + (wi & 1) * 8;
  f32x4 sx;
  auto step = [&](auto oddc, auto firstc, bool last) {
    constexpr bool ODD = decltype(oddc)::v, FIRST = decltype(firstc)::v;
    constexpr u32 SO = ODD ? 16384 : 0, SN = ODD ? 0 : 16384;
    bf16x8 kf[8], kx[4], qx[4];
    bf16x4 vl[8], vh[8];
#pragma unroll
    for (int f = 0; f < 3; ++f) kf[f] = kfc[f];
    f32x4 n0[3], n1[3];
    u32 pxw[2];
    u32x4 px;
    u32 kxa[2], qa[2], pw;
    auto vread = [&](int i, int hi) {
      const u32 a = va[((i >> 1) + 1) & 3] + ((i & 1) * 4096 + SO + hi * 2048);
      if (hi) vh[i] = ldstr(a); else vl[i] = ldstr(a);
    };
    auto vfr = [&](int i) { return __builtin_shufflevector(vl[i], vh[i], 0, 1, 2, 3, 4, 5, 6, 7); };
    SETPRIO(1);
    GAP();
    sfor<0, 50>([&](auto jc) {
      constexpr int j = decltype(jc)::v;
      if constexpr (j < 24) {
        constexpr int f = j / 3, q = j % 3, u = f & 3;
        if constexpr (f < 4) n0[q] = MFMA(kf[f], qf[q][u], u == 0 ? negm[q] : n0[q]);
        else n1[q] = MFMA(kf[f], qf[q][u], u == 0 ? negm[q] : n1[q]);
      } else if constexpr (j < 48) {
        constexpr int i = j < 42 ? (j - 24) / 3 : 6 + (j & 1), q = j < 42 ? (j - 24) % 3 : (j - 42) / 2;
        o[q][i] = MFMA(vfr(i), asb(p[q]), FIRST ? ZERO4 : o[q][i]);
      } else {
        ox[j - 48] = MFMA(vfr(j - 42), asb(px), FIRST ? ZERO4 : ox[j - 48]);
      }
      GAP();
      if constexpr (j == 0) SET_M0(lhk, SN);
      if constexpr (j == 1) DMA_LD(voff, rk2, so, 0);
      if constexpr (j == 3) SET_M0(lhk, SN + 3968);
      if constexpr (j == 4) DMA_LD(voff, rk2, so, 128);
      if constexpr (j < 15 && j % 3 == 2) kf[3 + j / 3] = kfrag(ka, 3 + j / 3, SO);
      if constexpr (j == 12) SETPRIO(0);
      if constexpr (j >= 16 && j < 36 && (j - 16) % 3 < 2) vread((j - 16) / 3, (j - 16) % 3);
      if constexpr (j == 36 || j == 37) vread(7, j - 36);
      if constexpr (j >= 43 && (j - 43) % 3 == 0) kfc[(j - 43) / 3] = kfrag(ka, (j - 43) / 3, SN);
      if constexpr (j == 40) { if constexpr (FIRST) px = px0; else px = *(const LAS u32x4*)(size_t)(pa + (ODD ? 0 : 1024)); }
      if constexpr (!FIRST) {
        if constexpr (j == (ODD ? 44 : 24)) SET_M0(lhv, SN);
        if constexpr (j == (ODD ? 45 : 27)) DMA_LD(voff, rv, so, 0);
        if constexpr (j == (ODD ? 47 : 30)) SET_M0(lhv, SN + 3968);
        if constexpr (j == (ODD ? 48 : 33)) DMA_LD(voff, rv, so, 128);
      }
      if constexpr (j == 48) { so += 8192; PINS(so); }
      if constexpr (ODD) {
        if constexpr (j == 18 && !FIRST) *(LAS u32x2*)(size_t)pw = u32x2{pxw[0], pxw[1]};
        if constexpr (j == 24) kx[0] = kfrag(kxa, 0, 0);
        if constexpr (j == 27) qx[0] = lds128(qa[0]);
        if constexpr (j == 30) kx[1] = kfrag(kxa, 1, 0);
        if constexpr (j == 33) qx[1] = lds128(qa[1]);
        if constexpr (j == 38) kx[2] = kfrag(kxa, 2, 0);
        if constexpr (j == 39) qx[2] = lds128(qa[0] + 2048);
        if constexpr (j == 41) kx[3] = kfrag(kxa, 3, 0);
        if constexpr (j == 42) qx[3] = lds128(qa[1] + 2048);
      } else {
        if constexpr (j == 38) SETPRIO(2);
        if constexpr (j == 39) { lhk ^= 32768u; PINS(lhk); }
        if constexpr (j == 41) { lhv ^= 32768u; PINS(lhv); }
        if constexpr (j == 42) { kxs = kxs + ((lhk & 32768u) ? (u32)-65536 : 65536u); PINS(kxs); }
        if constexpr (j == 44) { qs0 ^= 0x3800u; PINS(qs0); }
        if constexpr (j == 45) { qs1 ^= 0x3800u; PINS(qs1); }
      }
      GAP();
      auto vflip = [&](int jj) { if (FIRST) va[jj] += VRING + hh * 8192 + 16384 - (hh ? V0B : V0A); else va[jj] ^= 32768u; PIN(va[jj]); };
      if constexpr (j < 12) sm_ops<50 + j>(s0, s1, p, l);
      else sm_ops<j>(n0, n1, p, l);
      if constexpr (ODD) {
        if constexpr (!FIRST) {
          if constexpr (j == 7) { pw = pa + pwo; PIN(pw); }
          if constexpr (j == 12 || j == 13) { sx[j - 10] = __builtin_amdgcn_exp2f(sx[j - 10]); PIN(sx); }
          if constexpr (j == 15) { pxw[0] = cvtpk(sx[0], sx[1]); PIN(pxw[0]); }
          if constexpr (j == 17) { pxw[1] = cvtpk(sx[2], sx[3]); PIN(pxw[1]); }
          if constexpr (j == 19 || j == 21) { l[3] += sx[2 + (j - 19) / 2]; PIN(l[3]); }
        }
        if constexpr (j == 23) { kxa[0] = ka[0] + kxs; PIN(kxa[0]); }
        if constexpr (j == 25) { qa[0] = pa + qs0; PIN(qa[0]); }
        if constexpr (j == 27) { kxa[1] = ka[1] + kxs; PIN(kxa[1]); }
        if constexpr (j == 29) { qa[1] = pa + qs1; PIN(qa[1]); }
        if constexpr (j == 31 || j == 33) { ka[(j - 31) / 2] ^= 32768u; PIN(ka[(j - 31) / 2]); }
        if constexpr (j == 39 || j == 41) vflip((j - 39) / 2);
      } else {
        if constexpr (j == 12 || j == 13) { sx[j - 12] = __builtin_amdgcn_exp2f(sx[j - 12] - m12); PIN(sx); }
        if constexpr (j == 15 || j == 17) { sx[2 + (j - 15) / 2] -= m12; PIN(sx); }
        if constexpr (j == 19 || j == 21) { l[3] += sx[(j - 19) / 2]; PIN(l[3]); }
        if constexpr (j == 41) { pa ^= 2048u; PIN(pa); }
      }
      GAP();
      if constexpr (ODD && (j == 32 || j == 38 || j == 44 || j == 47)) {
        constexpr int u = j == 32 ? 0 : (j == 38 ? 1 : (j == 44 ? 2 : 3));
        sx = MFMA(kx[u], qx[u], u == 0 ? ZERO4 : sx);
        GAP();
        if constexpr (j == 38) { SETPRIO(2); GAP(); vflip(2); }
        if constexpr (j == 44) vflip(3);
        GAP();
      }
      if constexpr (j == 23) {
        if constexpr (FIRST) asm volatile("s_waitcnt vmcnt(2)\n\ts_barrier" ::: "memory");
        else if constexpr (ODD) {
          if (last) asm volatile("s_waitcnt vmcnt(0)" ::: "memory");
          asm volatile("s_waitcnt vmcnt(4) lgkmcnt(4)\n\ts_barrier" ::: "memory");
        } else asm volatile("s_waitcnt vmcnt(6)\n\ts_barrier" ::: "memory");
        SETPRIO(3);
        GAP();
      }
    });
#pragma unroll
    for (int q = 0; q < 3; ++q) s1[q] = n1[q];
  };
  step(IC<1>{}, IC<1>{}, false);
  STAMP(8);
#pragma unroll 1
  for (u32 t = 2; t < 16; t += 2) {
    step(IC<0>{}, IC<0>{}, false);
    step(IC<1>{}, IC<0>{}, t == 14);
  }
  STAMP(11);
  SETPRIO(0);
  la16 = (pa ^ 2048u) - p12s;
  P2 c2;
  {
    const u32 ln = la16 >> 4, m = (ln - 3) & 3, kq = xcd >> 1;
    const u32 sib = xcd + 8 * (b + 8 * ((hpl + 1 + m) & 3)), per = br + 2 * ((kq + 1 + m) & 3) + 8 * rr;
    c2.ldA = ln < 3 ? bid * 512 + 128 + ln * 4 : sib * 512;
    c2.stA = sib * 512 + 128 + ((2 - m) & 3) * 4;
    c2.ldB = ln < 3 ? bid * 512 + 256 + (ln * 8 + wid) * 4 : per * 512;
    c2.stB = per * 512 + 256 + (((2 - m) & 3) * 8 + wid) * 4;
    asm volatile("buffer_load_dword %0, %1, %2, 0 offen sc1" : "=v"(c2.nvA) : "v"(c2.ldA), "s"(rf));
    asm volatile("buffer_load_dword %0, %1, %2, 0 offen sc1" : "=v"(c2.nvB) : "v"(c2.ldB), "s"(rf));
  }
  const u32 kq = xcd >> 1;
  c2.ro = mk_rsrc(ows + ((size_t)(br * 8 + b) * 128 + kq * 32) * 13 * 512, 32 * 13312);
  c2.rw = mk_rsrc((br ? woB : woA) + (size_t)kq * 1024 * 512 + hpl * 128, 1024 * 1024 - hpl * 256);
  auto wo_tiles = [&](int i0, int i1) {
    const u32 ln = la16 >> 4, voffW = (ln >> 3) * 1024 + ((((ln & 7) >> 1) ^ (ln >> 4)) * 32) + (ln & 1) * 16;
#pragma unroll
    for (int i = i0; i < i1; ++i) dma_wo(lds0 + i * SL2 + 13312 + hh * 4096 + wi * 1024, voffW, c2.rw, ((hpl * 4 + ((i / 2 + b) & 3)) * 64 + (i & 1) * 32 + wi * 8) * 1024 + hh * 128);
  };
  u32x4 w[3][4];
  auto opack = [&](int q, int u, float inv) {
    const f32x4 a = o[q][IPOS[2 * u]] * inv, bb = o[q][IPOS[2 * u + 1]] * inv;
    const u32 a0 = cvtpk(a[0], a[1]), a1 = cvtpk(a[2], a[3]), b0 = cvtpk(bb[0], bb[1]), b1 = cvtpk(bb[2], bb[3]);
    w[q][u] = u32x4{odd ? b0 : a0, odd ? b1 : a1, odd ? a0 : b0, odd ? a1 : b1};
    __builtin_amdgcn_raw_buffer_store_b128(w[q][u], c2.ro, la16, ((hpl * 8 + hh * 4 + (u ^ (wi >> 1))) * 13 + wi * 3 + q) * 1024, 16);
  };
  wo_tiles(0, 3);
  {
    constexpr int ORD_[8] = {2, 4, 3, 5, 6, 0, 7, 1};
    bf16x8 vf[8];
    const u32x4 px = *(const LAS u32x4*)(size_t)(la16 + (p12s + 3072));
#pragma unroll
    for (int i = 0; i < 4; ++i) vf[i] = vfrag(va, ORD_[i], 0);
    GAP();
    sfor<50, 62>([&](auto pc) { sm_ops<decltype(pc)::v>(s0, s1, p, l); });
    GAP();
    bool bad = false;
    float inv[3];
    sfor<0, 10>([&](auto ic) {
      constexpr int k = decltype(ic)::v, i = ORD_[k & 7];
      if constexpr (k < 4) vf[k + 4] = vfrag(va, ORD_[k + 4], 0);
      if constexpr (k < 8) {
#pragma unroll
        for (int q = 0; q < 3; ++q) o[q][i] = MFMA(vf[k], asb(p[q]), o[q][i]);
        if constexpr (i >= 6) ox[i - 6] = MFMA(vf[k], asb(px), ox[i - 6]);
      }
      if constexpr (k < 2) {
#pragma unroll
        for (int q = 2 * k; q < 2 * k + 2; ++q) { l[q] = xsum_g(l[q]); bad = bad || !(l[q] < 1.0e15f); if (q < 3) inv[q] = 1.0f / l[q]; }
      }
      if constexpr (k == 2) {
        *(LAS float*)(size_t)(lds0 + FLG + hh * 256 + wi * 64 + ((la16 >> 2) & 60)) = l[3];
        *(LAS u32*)(size_t)(lds0 + FLG + 1024 + wid * 4) = __any(bad) ? 1u : 0u;
        if (wid == 0) *(LAS u32*)(size_t)(lds0 + FLG + 1056) = nonce;
      }
      if constexpr (k >= 3 && (k & 1)) {
        constexpr int u = k == 3 ? 1 : (k == 5 ? 3 : (k == 7 ? 0 : 2));
#pragma unroll
        for (int q = 0; q < 3; ++q) opack(q, u, inv[q]);
      }
      GAP();
    });
  }
  asm volatile("s_waitcnt vmcnt(12) lgkmcnt(0)\n\ts_barrier" : "+v"(c2.nvA), "+v"(c2.nvB)::"memory");
  const u32 lsa = lds0 + FLG + hh * 256 + ((la16 >> 2) & 60);
  l[3] = (*(const LAS float*)(size_t)lsa + *(const LAS float*)(size_t)(lsa + 64)) + (*(const LAS float*)(size_t)(lsa + 128) + *(const LAS float*)(size_t)(lsa + 192));
  const u32x4 fl0 = *(const LAS u32x4*)(size_t)(lds0 + FLG + 1024), fl1 = *(const LAS u32x4*)(size_t)(lds0 + FLG + 1040);
  const u32 redo = __builtin_amdgcn_readfirstlane((fl0[0] | fl0[1] | fl0[2] | fl0[3]) | (fl1[0] | fl1[1] | fl1[2] | fl1[3]));
  c2.nonce = __builtin_amdgcn_readfirstlane(*(const LAS u32*)(size_t)(lds0 + FLG + 1056));
  auto epilogue = [&]() {
    wo_tiles(3, 6);
    auto slot_of = [&](u32 u) { const u32 e = hh * 4 + u; return lds0 + ((((e >> 1) - b) & 3) * 2 + (e & 1)) * SL2; };
#pragma unroll
    for (int u = 0; u < 4; ++u) {
      const u32 a = slot_of(u ^ (wi >> 1)) + wi * 3072;
#pragma unroll
      for (int q = 0; q < 3; ++q) *(LAS u32x4*)(size_t)(la16 + a + q * 1024) = w[q][u];
    }
    const float inv = 1.0f / l[3];
#pragma unroll
    for (int hf = 0; hf < 2; ++hf) {
      const f32x4 a = ox[hf] * inv;
      *(LAS u32x2*)(size_t)(la16 + slot_of(hf * 2 + (wi >> 1)) + 12288 + (wi & 1) * 8) = u32x2{cvtpk(a[0], a[1]), cvtpk(a[2], a[3])};
    }
  };
  STAMP(12);
  if (!redo) epilogue();
  STAMP(13);
  u32 redo2 = redo;
  PINS(redo2);
  if (redo2) {
    float m[4];
#pragma unroll
    for (int q = 0; q < 4; ++q) {
      l[q] = 0.f;
      m[q] = -3.0e38f;
    }
#pragma unroll
    for (int q = 0; q < 3; ++q)
#pragma unroll
      for (int dt = 0; dt < 8; ++dt) o[q][dt] = ZERO4;
    ox[0] = ox[1] = ZERO4;
#pragma unroll 1
    for (int t = 0; t < 16; ++t) {
      WAIT_BAR(0);
      kv_piece(rk, t, kvl, 0); kv_piece(rk, t, kvl, 1);
      kv_piece(rv, t, kvl + VRING + 32768, 0); kv_piece(rv, t, kvl + VRING + 32768, 1);
      WAIT_BAR(0);
      safe_block<8, 0>(ka, va, qf[0], o[0], m[0], l[0]);
      safe_block<8, 0>(ka, va, qf[1], o[1], m[1], l[1]);
      safe_block<8, 0>(ka, va, qf[2], o[2], m[2], l[2]);
      bf16x8 qx[4];
      PIN(la16);
#pragma unroll
      for (int q = 0; q < 4; ++q) qx[q] = q12frag(q);
      safe_block<2, 6>(ka, va, qx, ox, m[3], l[3]);
    }
#pragma unroll
    for (int q = 0; q < 4; ++q) l[q] = xsum_g(l[q]);
    WAIT_BAR(0);
    PIN(la16);
    wo_tiles(0, 3);
#pragma unroll
    for (int q = 0; q < 3; ++q)
#pragma unroll
      for (int u = 0; u < 4; ++u) opack(q, u, 1.0f / l[q]);
    epilogue();
    asm volatile("s_waitcnt vmcnt(0)" ::: "memory");
  }
  asm volatile("s_waitcnt lgkmcnt(0)\n\ts_barrier" ::: "memory");
  STAMP(14);
  c2.rp = mk_rsrc((char*)ows + WS_PART, 64 * 13 * 4 * 8192);
  c2.rf = rf;
  c2.wid = wid; c2.lane = la16 >> 4; c2.kq = kq; c2.j = hpl; c2.b = b;
  c2.pbase = ((br * 8 + b) * 4 + hpl) * (13 * 4 * 8192);
  c2.outp = out + (size_t)(b * 208) * 1024 + br * 512 + hpl * 128;
  if (wid < 4) p2_body<7>(c2 ST2PASS);
  else p2_body<6>(c2 ST2PASS);
}

extern "C" void kernel_launch(void* const* d_in, const int* in_sizes, int n_in, void* d_out, int out_size, void* d_ws, size_t ws_size, hipStream_t stream) {
  (void)in_sizes; (void)n_in; (void)out_size; (void)ws_size;
  (void)hipFuncSetAttribute((const void*)k1_qattn, hipFuncAttributeMaxDynamicSharedMemorySize, 163840);
  k1_qattn<<<256, 512, 163840, stream>>>((const u16*)d_in[0], (const u16*)d_in[1], (const u16*)d_in[2], (const u16*)d_in[3],
                                         (const u16*)d_in[5], (const u16*)d_in[6], (const u16*)d_in[7], (const u16*)d_in[8],
                                         (const u16*)d_in[4], (const u16*)d_in[9], (u16*)d_ws, (u16*)d_out);
}
```

```cpp
#include <hip/hip_runtime.h>
#include <hip/hip_bf16.h>

typedef unsigned short u16;
typedef unsigned int u32;
typedef __bf16 bf16x8 __attribute__((ext_vector_type(8)));
typedef __bf16 bf16x4 __attribute__((ext_vector_type(4)));
typedef __bf16 bf16x2 __attribute__((ext_vector_type(2)));
typedef float f32x4 __attribute__((ext_vector_type(4)));
typedef float f32x2 __attribute__((ext_vector_type(2)));
typedef u32 u32x4 __attribute__((ext_vector_type(4)));
#define LAS __attribute__((address_space(3)))
#define DI __device__ __forceinline__
typedef __amdgpu_buffer_rsrc_t rsrc_t;

#define MFMA(a, b, c) __builtin_amdgcn_mfma_f32_16x16x32_bf16((a), (b), (c), 0, 0, 0)
#define WAIT_BAR(N) asm volatile("s_waitcnt vmcnt(" #N ") lgkmcnt(0)\n\ts_barrier" ::: "memory")

DI rsrc_t mk_rsrc(const void* p, u32 bytes) { return __builtin_amdgcn_make_buffer_rsrc((void*)p, (short)0, (int)bytes, 0x00020000); }
DI void dma_nt(u32 lds, u32 voff, rsrc_t rs, u32 soff) {
  asm volatile("s_mov_b32 m0, %0\n\ts_nop 0\n\tbuffer_load_dwordx4 %1, %2, %3 offen nt lds" ::"s"(lds), "v"(voff), "s"(rs), "s"(soff));
}
DI void dma_df(u32 lds, u32 voff, rsrc_t rs, u32 soff) {
  asm volatile("s_mov_b32 m0, %0\n\ts_nop 0\n\tbuffer_load_dwordx4 %1, %2, %3 offen lds" ::"s"(lds), "v"(voff), "s"(rs), "s"(soff));
}

DI bf16x8 lds128(u32 a) { return *(const LAS bf16x8*)(size_t)a; }
DI bf16x4 ldstr(u32 a) { return __builtin_amdgcn_ds_read_tr16_b64_v4bf16((LAS bf16x4*)(size_t)a); }
DI bf16x8 ldstr2(u32 a0, u32 a1) { bf16x4 lo = ldstr(a0), hi = ldstr(a1); return __builtin_shufflevector(lo, hi, 0, 1, 2, 3, 4, 5, 6, 7); }
DI u32 cvtpk(float lo, float hi) { f32x2 v = {lo, hi}; return __builtin_bit_cast(u32, __builtin_convertvector(v, bf16x2)); }
DI float xsum_g(float v) {
  auto a = __builtin_amdgcn_permlane16_swap(__float_as_uint(v), __float_as_uint(v), false, false);
  v = __uint_as_float(a[0]) + __uint_as_float(a[1]);
  auto b = __builtin_amdgcn_permlane32_swap(__float_as_uint(v), __float_as_uint(v), false, false);
  return __uint_as_float(b[0]) + __uint_as_float(b[1]);
}
DI float xmax_g(float v) {
  auto a = __builtin_amdgcn_permlane16_swap(__float_as_uint(v), __float_as_uint(v), false, false);
  v = fmaxf(__uint_as_float(a[0]), __uint_as_float(a[1]));
  auto b = __builtin_amdgcn_permlane32_swap(__float_as_uint(v), __float_as_uint(v), false, false);
  return fmaxf(__uint_as_float(b[0]), __uint_as_float(b[1]));
}

constexpr float QSCALE = 0.08838834764831845f * 1.4426950408889634f;
#define GAP() __builtin_amdgcn_sched_barrier(0)
#define PIN(x) asm volatile("" : "+v"(x))
#define ZERO4 (f32x4{0.f, 0.f, 0.f, 0.f})
template <int N> struct IC { static constexpr int v = N; };
template <int B, int E, class F> DI void sfor(F&& f) { if constexpr (B < E) { f(IC<B>{}); sfor<B + 1, E>(f); } }

constexpr u32 VRING = 65536, XBUF = 65536, XSZ = 26624, K0A = 131072, V0A = 147456, V0B = 122880, Q12 = 155648, P12 = 139264, FLG = 139264;
typedef u32 u32x2 __attribute__((ext_vector_type(2)));

DI float max8(const f32x4& a, const f32x4& b) { return fmaxf(fmaxf(fmaxf(a[0], a[1]), fmaxf(a[2], a[3])), fmaxf(fmaxf(b[0], b[1]), fmaxf(b[2], b[3]))); }
DI bf16x8 asb(const u32x4& w) { return __builtin_bit_cast(bf16x8, w); }
DI bf16x8 kfrag(const u32 (&ka)[2], int f, u32 off) { return lds128(ka[f & 1] + (((f >> 1) & 1) * 4096 + (f >> 2) * 2048 + off)); }
DI bf16x8 vfrag(const u32 (&va)[4], int i, u32 off) { const u32 a = va[((i >> 1) + 1) & 3] + ((i & 1) * 4096 + off); return ldstr2(a, a + 2048); }

template <int NT, int I0>
DI void safe_block(u32 (&ka)[2], u32 (&va)[4], const bf16x8 (&qf)[4], f32x4 (&o)[NT], float& m, float& l) {
  f32x4 s[2];
#pragma unroll
  for (int f = 0; f < 8; ++f) { s[f >> 2] = MFMA(kfrag(ka, f, 0), qf[f & 3], (f & 3) ? s[f >> 2] : ZERO4); GAP(); }
  const float mx = fmaxf(xmax_g(max8(s[0], s[1])), m);
  const float al = __builtin_amdgcn_exp2f(m - mx);
  m = mx;
  float e[8];
#pragma unroll
  for (int i = 0; i < 8; ++i) e[i] = __builtin_amdgcn_exp2f(s[i >> 2][i & 3] - mx);
  l = l * al + (((e[0] + e[1]) + (e[2] + e[3])) + ((e[4] + e[5]) + (e[6] + e[7])));
  u32x4 w = {cvtpk(e[0], e[1]), cvtpk(e[2], e[3]), cvtpk(e[4], e[5]), cvtpk(e[6], e[7])};
  GAP();
#pragma unroll
  for (int i = 0; i < NT; ++i) { o[i] *= al; PIN(o[i]); o[i] = MFMA(vfrag(va, I0 + i, 0), asb(w), o[i]); PIN(o[i]); GAP(); }
}

constexpr int EP(int k) { return 14 + 2 * k; }
constexpr int CW(int q, int w) { return w < 2 ? 43 + 2 * q : (q == 0 ? 49 + 2 * (w - 2) : (q == 1 ? 53 + 2 * (w - 2) : 59 + 2 * (w - 2))); }

template <int POS>
DI void sm_ops(f32x4 (&s0)[3], f32x4 (&s1)[3], u32x4 (&p)[3], float (&l)[4]) {
  sfor<0, 24>([&](auto kc) {
    constexpr int k = decltype(kc)::v, q = (k % 12) / 4, r = k & 3;
    if constexpr (EP(k) + 1 == POS) { l[q] += (k < 12) ? s0[q][r] : s1[q][r]; PIN(l[q]); }
    if constexpr (EP(k) == POS) {
      if constexpr (k < 12) { s0[q][r] = __builtin_amdgcn_exp2f(s0[q][r]); PIN(s0[q]); }
      else { s1[q][r] = __builtin_amdgcn_exp2f(s1[q][r]); PIN(s1[q]); }
    }
  });
  sfor<0, 12>([&](auto qc) {
    constexpr int q = decltype(qc)::v / 4, w = decltype(qc)::v & 3;
    if constexpr (CW(q, w) == POS) {
      if constexpr (w < 2) p[q][w] = cvtpk(s0[q][2 * w], s0[q][2 * w + 1]); else p[q][w] = cvtpk(s1[q][2 * w - 4], s1[q][2 * w - 3]);
      PIN(p[q]);
    }
  });
}
#define SET_M0(base, imm) asm volatile("s_add_u32 m0, %0, %1" ::"s"(base), "i"(imm) : "scc")
#define DMA_LD(voff, rs, soff, imm) asm volatile("buffer_load_dwordx4 %0, %1, %2 offen offset:%3 nt lds" ::"v"(voff), "s"(rs), "s"(soff), "i"(imm))
#define DMA_DF(voff, rs, soff, imm) asm volatile("buffer_load_dwordx4 %0, %1, %2 offen offset:%3 lds" ::"v"(voff), "s"(rs), "s"(soff), "i"(imm))
#define PINS(x) asm volatile("" : "+s"(x))

constexpr u32 OSL = 13312, WRG = 8 * OSL;
constexpr u32 WSL(int u) { constexpr u32 R[7] = {1, 2, 3, 5, 6, 0, 4}; return R[u % 7] * 8192; }
DI u32 pair_of(u32 p, u32 j, u32 b) { return (((j + (p >> 2)) & 3) << 2) | ((p + b) & 3); }
constexpr size_t WS_PART = 28u << 20, WS_FLG = 27u << 20;
constexpr u32 PS = 4096;
DI f32x4 bf4(u32 lo, u32 hi) { return f32x4{__uint_as_float(lo << 16), __uint_as_float(lo & 0xffff0000u), __uint_as_float(hi << 16), __uint_as_float(hi & 0xffff0000u)}; }
DI u32 flag_ld(rsrc_t rf, u32 off) { return __builtin_amdgcn_raw_buffer_load_b32(rf, off, 0, 16); }
DI void flag_st(rsrc_t rf, u32 off, u32 v) { __builtin_amdgcn_raw_buffer_store_b32(v, rf, off, 0, 16); }
DI bool flag_pass(rsrc_t rf, u32 ld, u32 st, u32& nv, u32 nonce, u32 lane) {
  const u32 v = flag_ld(rf, ld);
  const bool chg = lane >= 3 && lane < 6 && v != nv;
  if (chg) { nv = v; flag_st(rf, st, v); }
  return __all(lane >= 3 || v == nonce);
}
DI void flag_wait(rsrc_t rf, u32 ld, u32 st, u32& nv, u32 nonce, u32 lane) {
  for (u32 spin = 0; spin < (1u << 21); ++spin) {
    if (flag_pass(rf, ld, st, nv, nonce, lane)) break;
    __builtin_amdgcn_s_sleep(1);
  }
}
struct P2 { rsrc_t ro, rw, rp, rf, rout; u32 wid, lane, kq, j, b, pbase, nonce, ldA, stA, nvA, ldB, stB, nvB; };

template <int NB, int KQ>
DI void p2_tail(f32x4 (&acc)[NB][2], P2& c) {
  constexpr int B0 = (NB == 7) ? 0 : 7;
  const u32 lane = c.lane, la16 = lane * 16, g = lane >> 4;
#pragma unroll
  for (int q = 0; q < NB; ++q)
    if (((B0 + q) & 3) != KQ) {
      const u32x4 w = {cvtpk(acc[q][0][0], acc[q][0][1]), cvtpk(acc[q][0][2], acc[q][0][3]), cvtpk(acc[q][1][0], acc[q][1][1]), cvtpk(acc[q][1][2], acc[q][1][3])};
      __builtin_amdgcn_raw_buffer_store_b128(w, c.rp, la16, c.pbase + ((B0 + q) * 4 + KQ) * PS, 16);
    }
  asm volatile("s_waitcnt vmcnt(0)" ::: "memory");
  if (lane >= 3 && lane < 6) flag_st(c.rf, c.stB, c.nvB);
  if (c.wid == 0) flag_pass(c.rf, c.ldA, c.stA, c.nvA, c.nonce, lane);
  flag_wait(c.rf, c.ldB, c.stB, c.nvB, c.nonce, lane);
  asm volatile("" ::: "memory");
  u32x4 pv[2][3];
#pragma unroll
  for (int q = 0, n = 0; q < NB; ++q)
    if (((B0 + q) & 3) == KQ) {
#pragma unroll
      for (int s = 0; s < 3; ++s) pv[n][s] = __builtin_amdgcn_raw_buffer_load_b128(c.rp, la16, c.pbase + ((B0 + q) * 4 + ((KQ + 1 + s) & 3)) * PS, 16);
      ++n;
    }
  flag_pass(c.rf, c.ldB, c.stB, c.nvB, c.nonce, lane);
  const u32 cg = ((g & 1) << 1) | (g >> 1);
#pragma unroll
  for (int q = 0, n = 0; q < NB; ++q)
    if (((B0 + q) & 3) == KQ) {
      f32x4 r[2];
#pragma unroll
      for (int e = 0; e < 2; ++e) {
        f32x4 t[3];
#pragma unroll
        for (int s = 0; s < 3; ++s) t[s] = bf4(pv[n][s][2 * e], pv[n][s][2 * e + 1]);
        r[e] = (acc[q][e] + t[0]) + (t[1] + t[2]);
      }
      auto s0 = __builtin_amdgcn_permlane16_swap(cvtpk(r[0][0], r[0][1]), cvtpk(r[1][0], r[1][1]), false, false);
      auto s1 = __builtin_amdgcn_permlane16_swap(cvtpk(r[0][2], r[0][3]), cvtpk(r[1][2], r[1][3]), false, false);
      __builtin_amdgcn_raw_buffer_store_b128(u32x4{s0[0], s1[0], s0[1], s1[1]}, c.rout, (lane & 15) * 2048 + cg * 16, (B0 + q) * 32768 + (c.wid & 3) * 64, 16);
      ++n;
    }
}

template <int NB>
DI void p2_body(P2& c) {
  constexpr int YJ = 2;
  constexpr int B0 = (NB == 7) ? 0 : 7, HH = NB == 7 ? 0 : 1;
  const u32 lane = c.lane, wid = c.wid;
  const u32 g = lane >> 4, r16 = lane & 15, q4 = r16 >> 2, p4 = lane & 3;
  const u32 j3 = lane >> 3, j7 = lane & 7, j4 = lane >> 4;
  const u32 np = wid & 3, la16 = lane * 16;
  const u32 voffW = j3 * 1024 + (((j7 >> 1) ^ j4) * 32) + (lane & 1) * 16 + wid * 8192;
  const u32 voffO0 = la16 + wid * 1024, voffO1 = voffO0 + 8192, voffO2 = voffO0 + 16384;
  const u32 lwd = WRG + np * 1024, lo0 = wid * 1024;
  const u32 wa = WRG + (np >> 1) * 4096 + (4 * g + q4) * 128 + ((((q4 >> 1) | ((g & 1) << 1)) ^ ((np & 1) * 2)) * 32) + p4 * 8, wb = wa ^ 32u;
  const u32 oa0 = B0 * 1024 + la16, oa1 = oa0 + 4 * OSL;
  f32x4 acc[NB][2];
  bf16x4 wl[2], wh[2];
  bf16x8 oc[NB];
  u32 fv = 0;
  wl[0] = ldstr(wa + WSL(0)); wh[0] = ldstr(wa + WSL(0) + 2048); wl[1] = ldstr(wb + WSL(0)); wh[1] = ldstr(wb + WSL(0) + 2048);
#pragma unroll
  for (int q = 0; q < NB; ++q) oc[q] = lds128(oa0 + q * 1024);
  if (wid == 0 && lane >= 3 && lane < 6) flag_st(c.rf, c.stA, c.nvA);
  auto o_m0 = [&](int p, int i) { SET_M0(lo0, (p & 3) * 2 * OSL + i * 8192); };
  auto o_ld = [&](u32 so, int i) { if (i == 0) DMA_DF(voffO0, c.ro, so, 0); if (i == 1) DMA_DF(voffO1, c.ro, so, 0); if (i == 2) DMA_DF(voffO2, c.ro, so, 0); };
  auto o_4th = [&](int p, u32 so) { if (NB == 7 && wid < 2) dma_df(lo0 + (p & 3) * 2 * OSL + 24576, voffO0, c.ro, so + 24576); };
  sfor<0, 32>([&](auto uc) {
    constexpr int u = decltype(uc)::v, j = (u - 1) >> 1;
    constexpr bool WD = u >= 1 && j <= 12, OD = u >= 1 && j >= YJ && j <= 12;
    constexpr u32 ws_ = WSL(u + 1), os_ = ((u + 1) & 3) * OSL;
    bf16x8 on[NB];
    bf16x4 nl[2], nh[2];
    u32 sw, so;
    const u32 oan = ((u + 1) & 4) ? oa1 : oa0;
    const bf16x8 w0 = __builtin_shufflevector(wl[0], wh[0], 0, 1, 2, 3, 4, 5, 6, 7), w1 = __builtin_shufflevector(wl[1], wh[1], 0, 1, 2, 3, 4, 5, 6, 7);
    if constexpr (OD && !(u & 1)) { so = pair_of(j + 3, c.j, c.b) * (2 * OSL); }
    GAP();
    sfor<0, 2 * NB>([&](auto nc) {
      constexpr int n = decltype(nc)::v, q = n >> 1;
      if constexpr (n & 1) acc[q][1] = MFMA(w1, oc[q], u == 0 ? ZERO4 : acc[q][1]); else acc[q][0] = MFMA(w0, oc[q], u == 0 ? ZERO4 : acc[q][0]);
      GAP();
      if constexpr (n == 0 && (u & 1)) {
        constexpr int X = j == YJ ? 0 : (j == 0 || j == 15) ? 63 : j == 14 ? 0 : (j == YJ + 1) ? 5 : 2 + (j - 1 >= YJ ? 3 : 0);
        if constexpr (j == YJ) {
          if (wid == 0) {
            if (!__all(lane >= 3 || fv == c.nonce)) flag_wait(c.rf, c.ldA, c.stA, c.nvA, c.nonce, lane);
            __builtin_amdgcn_fence(__ATOMIC_ACQUIRE, "agent");
          }
        }
        asm volatile("s_waitcnt vmcnt(%0)\n\ts_barrier" ::"i"(X) : "memory");
        if constexpr (j == YJ) {
          sfor<4, YJ + 3>([&](auto pc) {
            constexpr int p = decltype(pc)::v;
            const u32 sp = pair_of(p, c.j, c.b) * (2 * OSL);
#pragma unroll
            for (int i = 0; i < 3; ++i) { o_m0(p, i); asm volatile("s_nop 0"); o_ld(sp, i); }
            o_4th(p, sp);
          });
        }
      }
      if constexpr (u < 31) {
        if constexpr (n == 1) nl[0] = ldstr(wa + ws_);
        if constexpr (n == 2) nh[0] = ldstr(wa + ws_ + 2048);
        if constexpr (n == 3) on[0] = lds128(oan + os_);
        if constexpr (n == 4) nl[1] = ldstr(wb + ws_);
        if constexpr (n == 5) nh[1] = ldstr(wb + ws_ + 2048);
        if constexpr (n >= 6 && n - 5 < NB) on[n - 5] = lds128(oan + os_ + (n - 5) * 1024);
      }
      if constexpr (WD && (u & 1)) {
        constexpr u32 lo = WSL(2 * j + 6 + HH);
        if constexpr (n == 1) { sw = pair_of(j + 3, c.j, c.b); PINS(sw); }
        if constexpr (n == 2) { so = sw * (2 * OSL); sw *= 65536; PINS(sw); PINS(so); SET_M0(lwd, lo); }
        if constexpr (n == 3) DMA_DF(voffW, c.rw, sw, 0);
        if constexpr (n == 6) SET_M0(lwd, lo + 3968);
        if constexpr (n == 7) DMA_DF(voffW, c.rw, sw, 128);
      }
      if constexpr (OD) {
        if constexpr ((u & 1) && n == 10) o_m0(j + 3, 0);
        if constexpr ((u & 1) && n == 11) o_ld(so, 0);
        if constexpr (!(u & 1) && (n == 2 || n == 6)) o_m0(j + 3, 1 + n / 4);
        if constexpr (!(u & 1) && (n == 3 || n == 7)) o_ld(so, 1 + n / 4);
        if constexpr (!(u & 1) && n == 10) o_4th(j + 3, so);
      }
      if constexpr (u == 2 * YJ && n == 8) { if (wid == 0) fv = flag_ld(c.rf, c.ldA); }
      GAP();
    });
    if constexpr (u < 31) {
#pragma unroll
      for (int q = 0; q < NB; ++q) oc[q] = on[q];
      wl[0] = nl[0]; wh[0] = nh[0]; wl[1] = nl[1]; wh[1] = nh[1];
    }
  });
  c.pbase += np * 1024;
  if (c.kq == 0) p2_tail<NB, 0>(acc, c);
  else if (c.kq == 1) p2_tail<NB, 1>(acc, c);
  else if (c.kq == 2) p2_tail<NB, 2>(acc, c);
  else p2_tail<NB, 3>(acc, c);
}

__global__ __launch_bounds__(512, 2) void attn_block_fused(const u16* __restrict__ xA, const u16* __restrict__ kA, const u16* __restrict__ vA, const u16* __restrict__ wqA,
                                                   const u16* __restrict__ xB, const u16* __restrict__ kB, const u16* __restrict__ vB, const u16* __restrict__ wqB,
                                                   const u16* __restrict__ woA, const u16* __restrict__ woB, u16* __restrict__ ows, u16* __restrict__ out) {
  extern __shared__ __attribute__((aligned(16))) char smem[];
  const u32 bid = blockIdx.x, xcd = bid & 7, rr = bid >> 3;
  const u32 br = xcd & 1, b = rr & 7, hpl = rr >> 3, hp = (xcd >> 1) * 4 + hpl;
  const u32 lane = threadIdx.x & 63;
  const u32 wid = __builtin_amdgcn_readfirstlane(threadIdx.x >> 6);
  const u32 hh = wid >> 2, wi = wid & 3, h = hp * 2 + hh;
  const u32 c0 = (b + hpl) & 3;
  auto chunk_of = [&](u32 c) { return (c & 4) | ((c + c0) & 3); };
  const u32 lds0 = (u32)(size_t)(LAS char*)smem;
  const rsrc_t rx = mk_rsrc((br ? xB : xA) + (size_t)b * 208 * 512, 208 * 512 * 2);
  const rsrc_t rw = mk_rsrc(br ? wqB : wqA, 512 * 4096 * 2);
  const rsrc_t rk = mk_rsrc((br ? kB : kA) + (size_t)(b * 32 + h) * 512 * 128, 512 * 128 * 2);
  const rsrc_t rv = mk_rsrc((br ? vB : vA) + (size_t)(b * 32 + h) * 512 * 128, 512 * 128 * 2);
  const rsrc_t rf = mk_rsrc((char*)ows + WS_FLG, 256 * 512);
  u32 inbox;
  asm volatile("s_nop 4\n\tbuffer_load_dword %0, %1, %2, 0 offen sc1" : "=v"(inbox) : "v"(bid * 512 + (lane < 28 ? (lane < 4 ? (lane ? 124 + lane * 4 : 0) : 240 + lane * 4) : 0)), "s"(rf));

  const u32 g = lane >> 4, r16 = lane & 15, q4 = r16 >> 2, p4 = lane & 3;
  const u32 j3 = lane >> 3, j7 = lane & 7, j4 = lane >> 4;
  const u32 voffX = j3 * 1024 + ((j7 ^ ((wid & 1) * 4 + j4)) * 16);
  const u32 voffW = j3 * 8192 + ((j7 ^ ((j4 & 1) | ((wid & 1) << 2))) * 16);
  const u32 voff = j3 * 256 + (((j7 >> 1) ^ j4) * 32) + (lane & 1) * 16;
  const u32 kvl = lds0 + hh * 8192 + wi * 1024;
  const u32 kvs = wi * 2048;
  const u32 ks0 = (rr + hh * 8) & 15;
  auto kv_piece = [&](rsrc_t rs, u32 step, u32 lb, int hf) { dma_nt(lb + hf * 4096, voff, rs, ((step + ks0) & 15) * 8192 + kvs + hf * 128); };
  const u32 swb = wi * 65536 + h * 256;
  u32 jw = 0, jx = 0, sw = c0 * 524288 + swb, lw = kvl + 16384;
  u32 sx_ = wid * 8192 + c0 * 128, lx = lds0 + XBUF + wid * 1024, lx3 = wid < 2 ? lx + 24576 : lds0 + Q12 + wid * 1024;
  const u32 dx3 = wid < 2 ? XSZ : 0;
  u32 voffXi[4];
#pragma unroll
  for (int i = 0; i < 4; ++i) voffXi[i] = voffX + i * 65536;
  auto w_m0 = [&](int hf) { SET_M0(lw, hf * 3968); };
  auto w_ld = [&](int hf) { DMA_DF(voffW, rw, sw, hf * 128); };
  auto w_adv = [&](int ph) { if (ph == 0) { ++jw; sw = chunk_of(jw >> 1) * 524288 + (jw & 1) * 262144 + swb; PINS(sw); PINS(jw); } else { lw = (lw + 16384) & 0xffffu; PINS(lw); } };
  auto x_m0 = [&](int i) { if (i < 3) SET_M0(lx, i * 8192); else SET_M0(lx3, 0); };
  auto x_ld = [&](int i) { DMA_DF(voffXi[i], rx, sx_, 0); };
  auto x_adv = [&](u32 c) {
    ++jx; sx_ = wid * 8192 + chunk_of(jx) * 128; PINS(jx);
    lx += (c & 1) ? (u32)-XSZ : XSZ; lx3 += (c & 1) ? (u32)-dx3 : dx3;
    PINS(sx_); PINS(lx); PINS(lx3);
  };
  auto nop1 = [&]() { asm volatile("s_nop 0"); };
  const u32 v0l = lds0 + (hh ? V0B : V0A) + wi * 1024;
  w_m0(0); nop1(); w_ld(0); w_m0(1); nop1(); w_ld(1); w_adv(0); w_adv(1);
#pragma unroll
  for (int i = 0; i < 4; ++i) { x_m0(i); nop1(); x_ld(i); }
  x_adv(0);
  w_m0(0); nop1(); w_ld(0); w_m0(1); nop1(); w_ld(1); w_adv(0); w_adv(1);

  u32 xa[3], wa[4];
  xa[0] = lds0 + XBUF + wi * 6144 + r16 * 128 + ((g ^ (r16 >> 1)) * 16);
  xa[1] = xa[0] ^ 64u;
  xa[2] = xa[0] + (4 - wi) * 6144;
  wa[0] = lds0 + 16384 + hh * 8192 + (8 * g + q4) * 128 + ((((2 * (p4 >> 1)) | (wi & 1) | ((wi >> 1) << 2)) ^ ((q4 >> 1) | ((g & 1) << 2))) * 16) + (p4 & 1) * 8;
#pragma unroll
  for (int j = 1; j < 4; ++j) wa[j] = wa[0] ^ (u32)(((j & 1) << 4) | ((j >> 1) << 6));
  f32x4 o[3][8], ox[2];
  auto wread = [&](int n, int hi) { return ldstr(wa[(((n & 7) >> 1) + 1) & 3] + ((n & 1) * 4096 + hi * 512)); };
  constexpr int IPOS[8] = {6, 0, 2, 4, 7, 1, 3, 5};
  constexpr int ORD_[8] = {2, 4, 3, 5, 6, 0, 7, 1};
  bf16x8 qf[3][4];
  auto qconv = [&](int q, int u) {
    const f32x4 a = o[q][IPOS[2 * u]] * QSCALE, bb = o[q][IPOS[2 * u + 1]] * QSCALE;
    auto s0 = __builtin_amdgcn_permlane16_swap(cvtpk(a[0], a[1]), cvtpk(bb[0], bb[1]), false, false);
    auto s1 = __builtin_amdgcn_permlane16_swap(cvtpk(a[2], a[3]), cvtpk(bb[2], bb[3]), false, false);
    qf[q][u] = asb(u32x4{s0[0], s1[0], s0[1], s1[1]});
    PIN(qf[q][u]);
  };
  bf16x8 xc[4];
  bf16x4 wcl[3], wch[3];
  auto qchunk = [&](auto kindc, u32 c) {
    constexpr int KIND = decltype(kindc)::v;
    bf16x8 xf[2][4];
    bf16x4 wl[19], wh[19];
    u32 x13;
#pragma unroll
    for (int q = 0; q < 4; ++q) xf[0][q] = xc[q];
#pragma unroll
    for (int q = 0; q < 3; ++q) { wl[q] = wcl[q]; wh[q] = wch[q]; }
    const u32 dw = (c & 1) ? (u32)-49152 : 16384u, dx = (c & 1) ? (u32)-XSZ : XSZ;
    GAP();
    sfor<0, 16>([&](auto nc) {
      constexpr int n = decltype(nc)::v, i = (KIND == 4 && n >= 8) ? ORD_[n & 7] : (n & 7);
      constexpr int n3 = (KIND == 4 && n + 3 >= 8) ? ORD_[(n + 3) & 7] : n + 3;
      constexpr bool RD = (n + 3 < 16 || KIND != 4);
      constexpr int CU = (KIND == 4 && n >= 11 && (n & 1)) ? (n == 11 ? 1 : (n == 13 ? 3 : 0)) : -1;
      const bf16x8 wf = __builtin_shufflevector(wl[n], wh[n], 0, 1, 2, 3, 4, 5, 6, 7);
      auto dma_op = [&](auto kc) {
        constexpr int z = decltype(kc)::v;
        auto two = [&](auto m0f, auto ldf) { if constexpr (z == 0) m0f(); else ldf(); };
        if constexpr (KIND == 0 && n < 4) {
          constexpr int y = 2 * n + z;
          if constexpr (y == 0) w_m0(0); if constexpr (y == 1) { w_ld(0); w_m0(1); } if constexpr (y == 2) { w_ld(1); w_adv(0); w_adv(1); x_m0(0); }
          if constexpr (y >= 3 && y < 6) { x_ld(y - 3); x_m0(y - 2); } if constexpr (y == 6) { x_ld(3); x_adv(1); }
        }
        if constexpr (KIND >= 1 && KIND <= 3) {
          if constexpr (n == 0 || n == 2) two([&] { x_m0(2 + n / 2); }, [&] { x_ld(2 + n / 2); });
          if constexpr (n == 3 && z == 0) x_adv(c + 1);
        }
        if constexpr (KIND == 1 && n == 3 && z == 1) { kv_piece(rk, 0, kvl + K0A, 0); kv_piece(rk, 0, kvl + K0A, 1); kv_piece(rv, 0, v0l, 0); kv_piece(rv, 0, v0l, 1); }
        if constexpr ((KIND <= 3 && (n == 4 || n == 6)) || (KIND <= 2 && (n == 12 || n == 14))) two([&] { w_m0((n >> 1) & 1); }, [&] { w_ld((n >> 1) & 1); });
        if constexpr ((KIND <= 3 && n == 7) || (KIND <= 2 && n == 15)) w_adv(z);
        if constexpr (KIND <= 2 && (n == 8 || n == 10)) two([&] { x_m0((n - 8) / 2); }, [&] { x_ld((n - 8) / 2); });
        if constexpr (z == 1) {
          if constexpr (KIND == 3 && (n == 8 || n == 10)) kv_piece(rv, 1, kvl + VRING, (n >> 1) & 1);
          if constexpr (KIND == 3 && (n == 12 || n == 14)) kv_piece(rk, 1, kvl + 16384, (n >> 1) & 1);
          if constexpr (KIND == 4 && (n == 4 || n == 5)) kv_piece(rk, 2, kvl + 32768, n & 1);
          if constexpr (KIND == 4 && n >= 6 && n < 10) kv_piece(rv, n / 2 - 1, kvl + VRING + (n / 2 - 2) * 16384, n & 1);
          if constexpr (KIND == 4 && (n == 12 || n == 14)) kv_piece(rk, 3, kvl + 49152, (n >> 1) & 1);
        }
      };
      o[0][i] = MFMA(wf, xf[n >> 3][0], (KIND == 0 && n < 8) ? ZERO4 : o[0][i]);
      GAP();
      if constexpr (RD) wl[n + 3] = wread(n3, 0);
      GAP();
      if constexpr (CU >= 0) { qconv(0, CU); GAP(); }
      o[1][i] = MFMA(wf, xf[n >> 3][1], (KIND == 0 && n < 8) ? ZERO4 : o[1][i]);
      GAP();
      if constexpr (RD) wh[n + 3] = wread(n3, 1);
      if constexpr (n < 2) xf[1][n] = lds128(xa[1] + n * 2048);
      if constexpr (KIND != 4 && n >= 12 && n < 15) xc[n - 12] = lds128(xa[0] + (n - 12) * 2048);
      dma_op(IC<0>{});
      GAP();
      if constexpr (CU >= 0) qconv(1, CU);
      if constexpr (n == 0) { x13 = xa[2] ^ 64u; PIN(x13); }
      if constexpr (n == 4 || (n == 12 && KIND != 4)) { wa[0] += (n == 4 ? dw : 16384u); wa[1] += (n == 4 ? dw : 16384u); PIN(wa[0]); PIN(wa[1]); }
      if constexpr (n == 6 && KIND != 4) { xa[0] += dx; xa[1] += dx; PIN(xa[0]); PIN(xa[1]); }
      GAP();
      o[2][i] = MFMA(wf, xf[n >> 3][2], (KIND == 0 && n < 8) ? ZERO4 : o[2][i]);
      GAP();
      if constexpr (n == 0) xf[1][2] = lds128(xa[1] + 4096);
      if constexpr (n == 1) xf[1][3] = lds128(x13);
      if constexpr (KIND != 4 && n == 15) xc[3] = lds128(xa[2]);
      dma_op(IC<1>{});
      GAP();
      if constexpr (n == 4 || (n == 12 && KIND != 4)) { wa[2] += (n == 4 ? dw : 16384u); wa[3] += (n == 4 ? dw : 16384u); PIN(wa[2]); PIN(wa[3]); }
      if constexpr (CU >= 0) qconv(2, CU);
      if constexpr (n == 6 && KIND != 4) { xa[2] += dx; PIN(xa[2]); }
      GAP();
      if constexpr (i >= 6) { ox[i - 6] = MFMA(wf, xf[n >> 3][3], (KIND == 0 && n < 8) ? ZERO4 : ox[i - 6]); GAP(); }
      if constexpr (n == 3 || n == 11) {
        if constexpr (KIND == 4 && n == 11) asm volatile("s_barrier" ::: "memory");
        else if constexpr (n == 11) asm volatile("s_waitcnt vmcnt(%0)\n\ts_barrier" ::"i"(4 + (KIND == 1 ? 4 : 0)) : "memory");
        else asm volatile("s_waitcnt vmcnt(%0) lgkmcnt(4)\n\ts_barrier" ::"i"((KIND != 4 ? 6 : 4) + (KIND == 1 ? 4 : 0)) : "memory");
        GAP();
      }
    });
#pragma unroll
    for (int q = 0; q < 3; ++q) { wcl[q] = wl[16 + q]; wch[q] = wh[16 + q]; }
  };
  asm volatile("s_waitcnt vmcnt(2) lgkmcnt(0)\n\ts_barrier" : "+v"(inbox)::"memory");
  u32 nonce = __builtin_amdgcn_readfirstlane(inbox) + 1;
  if (wid == 0) {
    while (__any(inbox == nonce)) ++nonce;
    if (lane == 0) flag_st(rf, bid * 512, nonce);
  }
#pragma unroll
  for (int q = 0; q < 3; ++q) { wcl[q] = wread(q, 0); wch[q] = wread(q, 1); xc[q] = lds128(xa[0] + q * 2048); }
  xc[3] = lds128(xa[2]);
  qchunk(IC<0>{}, 0);
  qchunk(IC<1>{}, 1);
#pragma unroll 1
  for (u32 c = 2; c < 6; ++c) qchunk(IC<2>{}, c);
  qchunk(IC<3>{}, 6);
  qchunk(IC<4>{}, 7);
  const bool odd = wi & 1;
  u32 la16 = lane * 16;
  const u32 q12s = lds0 + Q12 + hh * 4096 + (wi >> 1) * 1024, qx8 = (wi & 1) * 256;
  auto q12frag = [&](int u) { return lds128((la16 ^ qx8) + ((q12s ^ (u32)((u & 1) << 10)) + (u >> 1) * 2048)); };
  u32 ka[2], va[4];
  ka[0] = lds0 + hh * 8192 + r16 * 128 + ((((g >> 1) | ((wi >> 1) << 1)) ^ ((r16 >> 1) & 3)) * 32) + ((g ^ wi) & 1) * 16;
  ka[1] = ka[0] ^ 64u;
  const u32 p12s = lds0 + P12 + hh * 4096;
  f32x4 negm[3], s0[3], s1[3];
  float m12;
  u32x4 p[3] = {}, px0;
  float l[4] = {0.f, 0.f, 0.f, 0.f};
  {
    const u32 k0[2] = {ka[0] + K0A, ka[1] + K0A};
    constexpr int UO[4] = {1, 3, 0, 2};
    f32x4 t0, t1;
    bf16x8 kf[8], qx[4];
#pragma unroll
    for (int f = 0; f < 4; ++f) kf[UO[f]] = kfrag(k0, UO[f], 0);
    GAP();
#pragma unroll
    for (int hf = 0; hf < 2; ++hf) {
      const f32x4 a = ox[hf] * QSCALE;
      *(LAS u32x2*)(size_t)(q12s + hf * 2048 + ((2 * (g >> 1) + (wi & 1)) * 16 + r16) * 16 + (g & 1) * 8) = u32x2{cvtpk(a[0], a[1]), cvtpk(a[2], a[3])};
    }
    GAP();
    sfor<0, 12>([&](auto jc) {
      constexpr int j = decltype(jc)::v, f = j / 3, q = j % 3;
      s0[q] = MFMA(kf[UO[f]], qf[q][UO[f]], f ? s0[q] : ZERO4);
      GAP();
      if constexpr (q == 0) kf[4 + f] = kfrag(k0, 4 + f, 0);
      GAP();
      if constexpr (j < 6 && !(j & 1)) qconv(j / 2, 2);
      if constexpr (j == 6) {
        va[0] = lds0 + (hh ? V0B : V0A) - 16384 + (4 * g + q4) * 128 + ((((q4 >> 1) | ((g & 1) << 1)) ^ wi) * 32) + p4 * 8;
#pragma unroll
        for (int k = 1; k < 4; ++k) va[k] = va[0] ^ (u32)(k << 5);
      }
      GAP();
    });
    asm volatile("s_waitcnt lgkmcnt(0)\n\ts_barrier" ::: "memory");
#pragma unroll
    for (int f = 0; f < 4; ++f) qx[f] = q12frag(f);
    GAP();
#pragma unroll
    for (int q = 0; q < 3; ++q) {
      const float mx = xmax_g(fmaxf(fmaxf(s0[q][0], s0[q][1]), fmaxf(s0[q][2], s0[q][3])));
      negm[q] = f32x4{-mx, -mx, -mx, -mx};
      s0[q] -= mx;
      PIN(negm[q]); PIN(s0[q]);
    }
    GAP();
    sfor<0, 20>([&](auto jc) {
      constexpr int j = decltype(jc)::v;
      if constexpr (j < 4) t0 = MFMA(kf[j], qx[j], j ? t0 : ZERO4);
      else if constexpr (j < 8) t1 = MFMA(kf[j], qx[j - 4], j > 4 ? t1 : ZERO4);
      else { constexpr int f = (j - 8) / 3, q = (j - 8) % 3; s1[q] = MFMA(kf[4 + f], qf[q][f], f ? s1[q] : negm[q]); }
      GAP();
      sm_ops<14 + j>(s0, s1, p, l);
      if constexpr (j == 19) sfor<34, 38>([&](auto pc) { sm_ops<decltype(pc)::v>(s0, s1, p, l); });
      if constexpr (j == 9) { m12 = xmax_g(fmaxf(fmaxf(t0[0], t0[1]), fmaxf(t0[2], t0[3]))); PIN(m12); }
      if constexpr (j >= 11 && j < 19) { constexpr int i = j - 11; if constexpr (i < 4) { t0[i] = __builtin_amdgcn_exp2f(t0[i] - m12); PIN(t0); } else { t1[i - 4] = __builtin_amdgcn_exp2f(t1[i - 4] - m12); PIN(t1); } }
      GAP();
    });
    l[3] = wi == 0 ? ((t0[0] + t0[1]) + (t0[2] + t0[3])) + ((t1[0] + t1[1]) + (t1[2] + t1[3])) : 0.f;
    px0 = u32x4{cvtpk(t0[0], t0[1]), cvtpk(t0[2], t0[3]), cvtpk(t1[0], t1[1]), cvtpk(t1[2], t1[3])};
    sfor<38, 50>([&](auto pc) { sm_ops<decltype(pc)::v>(s0, s1, p, l); });
  }
  WAIT_BAR(8);
  const u32 kt12 = (wi & 1) * 2048;
  bf16x8 kfc[3];
#pragma unroll
  for (int f = 0; f < 3; ++f) kfc[f] = kfrag(ka, f, 16384);
  if (wi < 2) {
    const u32 kx[2] = {ka[0] + kt12, ka[1] + kt12};
    f32x4 sy;
#pragma unroll
    for (int u = 0; u < 4; ++u) sy = MFMA(kfrag(kx, u, 16384), q12frag(u), u ? sy : ZERO4);
#pragma unroll
    for (int r = 0; r < 4; ++r) sy[r] = __builtin_amdgcn_exp2f(sy[r] - m12);
    l[3] += (sy[0] + sy[1]) + (sy[2] + sy[3]);
    *(LAS u32x2*)(size_t)(la16 + (p12s + 1024 + (wi & 1) * 8)) = u32x2{cvtpk(sy[0], sy[1]), cvtpk(sy[2], sy[3])};
  }
  u32 tt = 3, sok = ((4 + ks0) & 15) * 8192 + kvs;
  u32 so = ((3 + ks0) & 15) * 8192 + kvs;
  u32 lhk = kvl, lhv = kvl + VRING + 32768;
  u32 kxs = 32768 + kt12 + (wi >> 1) * 16384;
  u32 qs0 = q12s - p12s, qs1 = (q12s ^ 1024u) - p12s;
  u32 pa = la16 + p12s;
  const u32 pwo = (wi >> 1) * 1024 + (wi & 1) * 8;
  f32x4 sx;
  auto step = [&](auto oddc, auto firstc, bool last) {
    constexpr bool ODD = decltype(oddc)::v, FIRST = decltype(firstc)::v;
    constexpr u32 SO = ODD ? 16384 : 0, SN = ODD ? 0 : 16384;
    bf16x8 kf[8], kx[4], qx[4];
    bf16x4 vl[8], vh[8];
#pragma unroll
    for (int f = 0; f < 3; ++f) kf[f] = kfc[f];
    f32x4 n0[3], n1[3];
    u32 pxw[2];
    u32x4 px;
    u32 kxa[2], qa[2], pw;
    auto vread = [&](int i, int hi) {
      const u32 a = va[((i >> 1) + 1) & 3] + ((i & 1) * 4096 + SO + hi * 2048);
      if (hi) vh[i] = ldstr(a); else vl[i] = ldstr(a);
    };
    auto vfr = [&](int i) { return __builtin_shufflevector(vl[i], vh[i], 0, 1, 2, 3, 4, 5, 6, 7); };
    GAP();
    sfor<0, 50>([&](auto jc) {
      constexpr int j = decltype(jc)::v;
      if constexpr (j < 24) {
        constexpr int f = j / 3, q = j % 3, u = f & 3;
        if constexpr (f < 4) n0[q] = MFMA(kf[f], qf[q][u], u == 0 ? negm[q] : n0[q]);
        else n1[q] = MFMA(kf[f], qf[q][u], u == 0 ? negm[q] : n1[q]);
      } else if constexpr (j < 48) {
        constexpr int i = j < 42 ? (j - 24) / 3 : 6 + (j & 1), q = j < 42 ? (j - 24) % 3 : (j - 42) / 2;
        o[q][i] = MFMA(vfr(i), asb(p[q]), FIRST ? ZERO4 : o[q][i]);
      } else {
        ox[j - 48] = MFMA(vfr(j - 42), asb(px), FIRST ? ZERO4 : ox[j - 48]);
      }
      GAP();
      if constexpr (j == 0) SET_M0(lhk, SN);
      if constexpr (j == 1) DMA_LD(voff, rk, sok, 0);
      if constexpr (j == 3) SET_M0(lhk, SN + 3968);
      if constexpr (j == 4) DMA_LD(voff, rk, sok, 128);
      if constexpr (j < 15 && j % 3 == 2) kf[3 + j / 3] = kfrag(ka, 3 + j / 3, SO);
      if constexpr (j >= 16 && j < 36 && (j - 16) % 3 < 2) vread((j - 16) / 3, (j - 16) % 3);
      if constexpr (j == 36 || j == 37) vread(7, j - 36);
      if constexpr (j >= 43 && (j - 43) % 3 == 0) kfc[(j - 43) / 3] = kfrag(ka, (j - 43) / 3, SN);
      if constexpr (j == 40) { if constexpr (FIRST) px = px0; else px = *(const LAS u32x4*)(size_t)(pa + (ODD ? 0 : 1024)); }
      if constexpr (!FIRST) {
        if constexpr (j == (ODD ? 44 : 24)) SET_M0(lhv, SN);
        if constexpr (j == (ODD ? 45 : 27)) DMA_LD(voff, rv, so, 0);
        if constexpr (j == (ODD ? 47 : 30)) SET_M0(lhv, SN + 3968);
        if constexpr (j == (ODD ? 48 : 33)) DMA_LD(voff, rv, so, 128);
      }
      if constexpr (j == 48) { so = sok; ++tt; PINS(so); PINS(tt); }
      if constexpr (j == 49) { sok = tt < 15 ? ((tt + 1 + ks0) & 15) * 8192 + kvs : (1u << 20); PINS(sok); }
      if constexpr (ODD) {
        if constexpr (j == 18 && !FIRST) *(LAS u32x2*)(size_t)pw = u32x2{pxw[0], pxw[1]};
        if constexpr (j == 24) kx[0] = kfrag(kxa, 0, 0);
        if constexpr (j == 27) qx[0] = lds128(qa[0]);
        if constexpr (j == 30) kx[1] = kfrag(kxa, 1, 0);
        if constexpr (j == 33) qx[1] = lds128(qa[1]);
        if constexpr (j == 38) kx[2] = kfrag(kxa, 2, 0);
        if constexpr (j == 39) qx[2] = lds128(qa[0] + 2048);
        if constexpr (j == 41) kx[3] = kfrag(kxa, 3, 0);
        if constexpr (j == 42) qx[3] = lds128(qa[1] + 2048);
      } else {
        if constexpr (j == 39) { lhk ^= 32768u; PINS(lhk); }
        if constexpr (j == 41) { lhv ^= 32768u; PINS(lhv); }
        if constexpr (j == 42) { kxs = kxs + ((lhk & 32768u) ? (u32)-65536 : 65536u); PINS(kxs); }
        if constexpr (j == 44) { qs0 ^= 0x7800u; PINS(qs0); }
        if constexpr (j == 45) { qs1 ^= 0x7800u; PINS(qs1); }
      }
      GAP();
      auto vflip = [&](int jj) { if (FIRST) va[jj] += VRING + hh * 8192 + 16384 - (hh ? V0B : V0A); else va[jj] ^= 32768u; PIN(va[jj]); };
      if constexpr (j < 12) sm_ops<50 + j>(s0, s1, p, l);
      else sm_ops<j>(n0, n1, p, l);
      if constexpr (ODD) {
        if constexpr (!FIRST) {
          if constexpr (j == 7) { pw = pa + pwo; PIN(pw); }
          if constexpr (j == 12 || j == 13) { sx[j - 10] = __builtin_amdgcn_exp2f(sx[j - 10]); PIN(sx); }
          if constexpr (j == 15) { pxw[0] = cvtpk(sx[0], sx[1]); PIN(pxw[0]); }
          if constexpr (j == 17) { pxw[1] = cvtpk(sx[2], sx[3]); PIN(pxw[1]); }
          if constexpr (j == 19 || j == 21) { l[3] += sx[2 + (j - 19) / 2]; PIN(l[3]); }
        }
        if constexpr (j == 23) { kxa[0] = ka[0] + kxs; PIN(kxa[0]); }
        if constexpr (j == 25) { qa[0] = (pa ^ qx8) + qs0; PIN(qa[0]); }
        if constexpr (j == 27) { kxa[1] = ka[1] + kxs; PIN(kxa[1]); }
        if constexpr (j == 29) { qa[1] = qa[0] + (qs1 - qs0); PIN(qa[1]); }
        if constexpr (j == 31 || j == 33) { ka[(j - 31) / 2] ^= 32768u; PIN(ka[(j - 31) / 2]); }
        if constexpr (j == 39 || j == 41) vflip((j - 39) / 2);
      } else {
        if constexpr (j == 12 || j == 13) { sx[j - 12] = __builtin_amdgcn_exp2f(sx[j - 12] - m12); PIN(sx); }
        if constexpr (j == 15 || j == 17) { sx[2 + (j - 15) / 2] -= m12; PIN(sx); }
        if constexpr (j == 19 || j == 21) { l[3] += sx[(j - 19) / 2]; PIN(l[3]); }
        if constexpr (j == 41) { pa ^= 2048u; PIN(pa); }
      }
      GAP();
      if constexpr (ODD && (j == 32 || j == 38 || j == 44 || j == 47)) {
        constexpr int u = j == 32 ? 0 : (j == 38 ? 1 : (j == 44 ? 2 : 3));
        sx = MFMA(kx[u], qx[u], u == 0 ? ZERO4 : sx);
        GAP();
        if constexpr (j == 38) vflip(2);
        if constexpr (j == 44) vflip(3);
        GAP();
      }
      if constexpr (j == 23) {
        if constexpr (FIRST) asm volatile("s_waitcnt vmcnt(2)\n\ts_barrier" ::: "memory");
        else if constexpr (ODD) {
          if (last) asm volatile("s_waitcnt vmcnt(0)" ::: "memory");
          asm volatile("s_waitcnt vmcnt(4) lgkmcnt(4)\n\ts_barrier" ::: "memory");
        } else asm volatile("s_waitcnt vmcnt(6)\n\ts_barrier" ::: "memory");
        GAP();
      }
    });
#pragma unroll
    for (int q = 0; q < 3; ++q) s1[q] = n1[q];
  };
  step(IC<1>{}, IC<1>{}, false);
#pragma unroll 1
  for (u32 t = 2; t < 16; t += 2) {
    step(IC<0>{}, IC<0>{}, false);
    step(IC<1>{}, IC<0>{}, t == 14);
  }
  la16 = (pa ^ 2048u) - p12s;
  P2 c2;
  {
    const u32 ln = la16 >> 4, m = (ln - 3) & 3, kq = xcd >> 1;
    const u32 sib = xcd + 8 * (b + 8 * ((hpl + 1 + m) & 3)), per = br + 2 * ((kq + 1 + m) & 3) + 8 * rr;
    c2.ldA = ln < 3 ? bid * 512 + 128 + ln * 4 : sib * 512;
    c2.stA = sib * 512 + 128 + ((2 - m) & 3) * 4;
    c2.ldB = ln < 3 ? bid * 512 + 256 + (ln * 8 + wid) * 4 : per * 512;
    c2.stB = per * 512 + 256 + (((2 - m) & 3) * 8 + wid) * 4;
    asm volatile("buffer_load_dword %0, %1, %2, 0 offen sc1" : "=v"(c2.nvA) : "v"(c2.ldA), "s"(rf));
    asm volatile("buffer_load_dword %0, %1, %2, 0 offen sc1" : "=v"(c2.nvB) : "v"(c2.ldB), "s"(rf));
  }
  const u32 kq = xcd >> 1;
  c2.ro = mk_rsrc(ows + ((size_t)(br * 8 + b) * 128 + kq * 32) * 13 * 512, 32 * 13312);
  c2.rw = mk_rsrc((br ? woB : woA) + (size_t)kq * 1024 * 512 + hpl * 128, 1024 * 1024 - hpl * 256);
  auto wo_tiles = [&](int i0, int i1) {
    const u32 ln = la16 >> 4, voffW = (ln >> 3) * 1024 + ((((ln & 7) >> 1) ^ (ln >> 4)) * 32) + (ln & 1) * 16;
#pragma unroll
    for (int i = i0; i < i1; ++i) dma_df(lds0 + WRG + WSL(i) + hh * 4096 + wi * 1024, voffW, c2.rw, ((hpl * 4 + ((i / 2 + b) & 3)) * 64 + (i & 1) * 32 + wi * 8) * 1024 + hh * 128);
  };
  u32x4 w[3][4];
  auto opack = [&](int q, int u, float inv) {
    const f32x4 a = o[q][IPOS[2 * u]] * inv, bb = o[q][IPOS[2 * u + 1]] * inv;
    const u32 a0 = cvtpk(a[0], a[1]), a1 = cvtpk(a[2], a[3]), b0 = cvtpk(bb[0], bb[1]), b1 = cvtpk(bb[2], bb[3]);
    w[q][u] = u32x4{odd ? b0 : a0, odd ? b1 : a1, odd ? a0 : b0, odd ? a1 : b1};
    __builtin_amdgcn_raw_buffer_store_b128(w[q][u], c2.ro, la16, ((hpl * 8 + hh * 4 + (u ^ (wi >> 1))) * 13 + wi * 3 + q) * 1024, 16);
  };
  wo_tiles(0, 4);
  {
    bf16x8 vf[8];
    const u32x4 px = *(const LAS u32x4*)(size_t)(la16 + (p12s + 3072));
#pragma unroll
    for (int i = 0; i < 4; ++i) vf[i] = vfrag(va, ORD_[i], 0);
    GAP();
    sfor<50, 62>([&](auto pc) { sm_ops<decltype(pc)::v>(s0, s1, p, l); });
    GAP();
    bool bad = false;
    float inv[3];
    sfor<0, 10>([&](auto ic) {
      constexpr int k = decltype(ic)::v, i = ORD_[k & 7];
      if constexpr (k < 4) vf[k + 4] = vfrag(va, ORD_[k + 4], 0);
      if constexpr (k < 8) {
#pragma unroll
        for (int q = 0; q < 3; ++q) o[q][i] = MFMA(vf[k], asb(p[q]), o[q][i]);
        if constexpr (i >= 6) ox[i - 6] = MFMA(vf[k], asb(px), ox[i - 6]);
      }
      if constexpr (k < 2) {
#pragma unroll
        for (int q = 2 * k; q < 2 * k + 2; ++q) { l[q] = xsum_g(l[q]); bad = bad || !(l[q] < 1.0e15f); if (q < 3) inv[q] = 1.0f / l[q]; }
      }
      if constexpr (k == 2) {
        *(LAS float*)(size_t)(lds0 + FLG + hh * 256 + wi * 64 + ((la16 >> 2) & 60)) = l[3];
        *(LAS u32*)(size_t)(lds0 + FLG + 1024 + wid * 4) = __any(bad) ? 1u : 0u;
        if (wid == 0) *(LAS u32*)(size_t)(lds0 + FLG + 1056) = nonce;
      }
      if constexpr (k >= 3 && (k & 1)) {
        constexpr int u = k == 3 ? 1 : (k == 5 ? 3 : (k == 7 ? 0 : 2));
#pragma unroll
        for (int q = 0; q < 3; ++q) opack(q, u, inv[q]);
      }
      GAP();
    });
  }
  asm volatile("s_waitcnt vmcnt(12) lgkmcnt(0)\n\ts_barrier" : "+v"(c2.nvA), "+v"(c2.nvB)::"memory");
  const u32 lsa = lds0 + FLG + hh * 256 + ((la16 >> 2) & 60);
  l[3] = (*(const LAS float*)(size_t)lsa + *(const LAS float*)(size_t)(lsa + 64)) + (*(const LAS float*)(size_t)(lsa + 128) + *(const LAS float*)(size_t)(lsa + 192));
  const u32x4 fl0 = *(const LAS u32x4*)(size_t)(lds0 + FLG + 1024), fl1 = *(const LAS u32x4*)(size_t)(lds0 + FLG + 1040);
  const u32 redo = __builtin_amdgcn_readfirstlane((fl0[0] | fl0[1] | fl0[2] | fl0[3]) | (fl1[0] | fl1[1] | fl1[2] | fl1[3]));
  c2.nonce = __builtin_amdgcn_readfirstlane(*(const LAS u32*)(size_t)(lds0 + FLG + 1056));
  auto epilogue = [&]() {
    auto slot_of = [&](u32 u) { const u32 e = hh * 4 + u; return lds0 + ((((e >> 1) - b) & 3) * 2 + (e & 1)) * OSL; };
    const float inv = 1.0f / l[3];
#pragma unroll
    for (int hf = 0; hf < 2; ++hf) {
      const f32x4 a = ox[hf] * inv;
      const u32x2 wx = {cvtpk(a[0], a[1]), cvtpk(a[2], a[3])};
      __builtin_amdgcn_raw_buffer_store_b64(wx, c2.ro, la16, ((hpl * 8 + hh * 4 + hf * 2 + (wi >> 1)) * 13 + 12) * 1024 + (wi & 1) * 8, 16);
      *(LAS u32x2*)(size_t)(la16 + slot_of(hf * 2 + (wi >> 1)) + 12288 + (wi & 1) * 8) = wx;
    }
    wo_tiles(4, 6);
#pragma unroll
    for (int u = 0; u < 4; ++u) {
      const u32 a = slot_of(u ^ (wi >> 1)) + wi * 3072;
#pragma unroll
      for (int q = 0; q < 3; ++q) *(LAS u32x4*)(size_t)(la16 + a + q * 1024) = w[q][u];
    }
  };
  if (!redo) epilogue();
  u32 redo2 = redo;
  PINS(redo2);
  if (redo2) {
    float m[4];
#pragma unroll
    for (int q = 0; q < 4; ++q) {
      l[q] = 0.f;
      m[q] = -3.0e38f;
    }
#pragma unroll
    for (int q = 0; q < 3; ++q)
#pragma unroll
      for (int dt = 0; dt < 8; ++dt) o[q][dt] = ZERO4;
    ox[0] = ox[1] = ZERO4;
#pragma unroll 1
    for (int t = 0; t < 16; ++t) {
      WAIT_BAR(0);
      kv_piece(rk, t, kvl, 0); kv_piece(rk, t, kvl, 1);
      kv_piece(rv, t, kvl + VRING + 32768, 0); kv_piece(rv, t, kvl + VRING + 32768, 1);
      WAIT_BAR(0);
      safe_block<8, 0>(ka, va, qf[0], o[0], m[0], l[0]);
      safe_block<8, 0>(ka, va, qf[1], o[1], m[1], l[1]);
      safe_block<8, 0>(ka, va, qf[2], o[2], m[2], l[2]);
      bf16x8 qx[4];
      PIN(la16);
#pragma unroll
      for (int q = 0; q < 4; ++q) qx[q] = q12frag(q);
      safe_block<2, 6>(ka, va, qx, ox, m[3], l[3]);
    }
#pragma unroll
    for (int q = 0; q < 4; ++q) l[q] = xsum_g(l[q]);
    WAIT_BAR(0);
    PIN(la16);
    wo_tiles(0, 4);
#pragma unroll
    for (int q = 0; q < 3; ++q)
#pragma unroll
      for (int u = 0; u < 4; ++u) opack(q, u, 1.0f / l[q]);
    epilogue();
    asm volatile("s_waitcnt vmcnt(0)" ::: "memory");
  }
  asm volatile("s_waitcnt vmcnt(2) lgkmcnt(0)\n\ts_barrier" ::: "memory");
  c2.rp = mk_rsrc((char*)ows + WS_PART, 64 * 13 * 4 * PS);
  c2.rf = rf;
  c2.wid = wid; c2.lane = la16 >> 4; c2.kq = kq; c2.j = hpl; c2.b = b;
  c2.pbase = ((br * 8 + b) * 4 + hpl) * (13 * 4 * PS);
  c2.rout = mk_rsrc(out + (size_t)(b * 208) * 1024 + br * 512 + hpl * 128, 208 * 2048 - br * 1024 - hpl * 256);
  if (wid < 4) p2_body<7>(c2);
  else p2_body<6>(c2);
}

extern "C" void kernel_launch(void* const* d_in, const int* in_sizes, int n_in, void* d_out, int out_size, void* d_ws, size_t ws_size, hipStream_t stream) {
  (void)in_sizes; (void)n_in; (void)out_size; (void)ws_size;
  (void)hipFuncSetAttribute((const void*)attn_block_fused, hipFuncAttributeMaxDynamicSharedMemorySize, 163840);
  attn_block_fused<<<256, 512, 163840, stream>>>((const u16*)d_in[0], (const u16*)d_in[1], (const u16*)d_in[2], (const u16*)d_in[3],
                                         (const u16*)d_in[5], (const u16*)d_in[6], (const u16*)d_in[7], (const u16*)d_in[8],
                                         (const u16*)d_in[4], (const u16*)d_in[9], (u16*)d_ws, (u16*)d_out);
}
```
